```python
import torch

def _softmax(x, axis=-1):
    return torch.softmax(x, dim=axis)
def _gelu(x):
    return torch.nn.functional.gelu(x, approximate="tanh")
def _silu(x):
    return torch.nn.functional.silu(x)
def _sigmoid(x):
    return torch.sigmoid(x)
def _layernorm(x, axis=-1, eps=1e-5):
    m = x.mean(dim=axis, keepdim=True)
    v = x.var(dim=axis, keepdim=True, unbiased=False)
    return (x - m) / torch.sqrt(v + eps)
def _scatter_add(tgt, idx, src, axis=0):
    return tgt.clone().index_add_(axis, idx, src)
def _segment_sum(src, seg, n):
    out = torch.zeros((n,) + src.shape[1:], dtype=src.dtype)
    return out.index_add_(0, seg, src)
def _pad(x, pw):
    flat = tuple(v for pair in reversed(pw) for v in pair)
    return torch.nn.functional.pad(x, flat)

kernel_name = 'gated_mix__sliding_local_dilated__spectral_fnet__prompt'

def setup_inputs():

    _w = (lambda a: (lambda t: t.to(torch.bfloat16) if t.is_floating_point() else t)(torch.as_tensor(a)))
    def _A_setup():
        B, S, D = 1, 2048, 1024
        return {
            "x": _w((lambda *s: torch.randn(*s, dtype=torch.bfloat16) * 0.05)(B, S, D)),
            "wqkv": _w((lambda *s: torch.randn(*s, dtype=torch.bfloat16) * 1.6)(D, 3 * D)),
            "wo": _w((lambda *s: torch.randn(*s, dtype=torch.bfloat16) * 0.2)(D, D)),
        }
    _A_in = _A_setup()

    def _B_setup():
        B, S, D = 1, 2048, 1024
        ns = torch.arange(S)[:, None] * torch.arange(S)[None, :]
        nd = torch.arange(D)[:, None] * torch.arange(D)[None, :]
        Ws_re = torch.cos(-2.0 * torch.pi * ns / S).to(float)
        Ws_im = torch.sin(-2.0 * torch.pi * ns / S).to(float)
        Wd_re = torch.cos(-2.0 * torch.pi * nd / D).to(float)
        Wd_im = torch.sin(-2.0 * torch.pi * nd / D).to(float)
        return {
            "x": _w((lambda *s: torch.randn(*s, dtype=torch.bfloat16) * 0.05)(B, S, D)),
            "dft_seq_re": _w(Ws_re),
            "dft_seq_im": _w(Ws_im),
            "dft_hid_re": _w(Wd_re),
            "dft_hid_im": _w(Wd_im),
        }
    _B_in = _B_setup()

    return {
        "A_x": _A_in["x"],
        "A_wqkv": _A_in["wqkv"],
        "A_wo": _A_in["wo"],
        "B_x": _B_in["x"],
        "B_dft_seq_re": _B_in["dft_seq_re"],
        "B_dft_seq_im": _B_in["dft_seq_im"],
        "B_dft_hid_re": _B_in["dft_hid_re"],
        "B_dft_hid_im": _B_in["dft_hid_im"],
        "gate_w": _w((lambda *s: torch.randn(*s, dtype=torch.bfloat16) * 0.05)(1024) * 16.0),
    }

def reference(A_x, A_wqkv, A_wo, B_x, B_dft_seq_re, B_dft_seq_im, B_dft_hid_re, B_dft_hid_im, gate_w):
    def _A_ref(x, wqkv, wo):
        B, S, D = x.shape
        H = 32; Dh = D // H
        W, dil = 8, 2
        qkv = (lambda x, *d: x.permute(*d))((x @ wqkv).reshape(B, S, 3, H, Dh), 2, 0, 3, 1, 4)
        q, k, v = qkv[0], qkv[1], qkv[2]
        scale = Dh ** -0.5
        pos = torch.arange(S)
        off = torch.arange(W) * dil
        idx = pos[:, None] - off[None, :]
        mask = idx >= 0
        idx_c = torch.where(mask, idx, 0).reshape(-1)
        kg = k[:, :, idx_c, :].reshape(B, H, S, W, Dh)
        vg = v[:, :, idx_c, :].reshape(B, H, S, W, Dh)
        logits = torch.einsum("bhsd,bhswd->bhsw", q, kg) * scale
        logits = torch.where(mask[None, None, :, :], logits, -1e9)
        attn = _softmax(logits, axis=-1)
        o = torch.einsum("bhsw,bhswd->bhsd", attn, vg)
        return (lambda x, *d: x.permute(*d))(o, 0, 2, 1, 3).reshape(B, S, D) @ wo

    def _B_ref(x, dft_seq_re, dft_seq_im, dft_hid_re, dft_hid_im):
        a = torch.einsum("bsd,ed->bse", x, dft_hid_re)
        b = torch.einsum("bsd,ed->bse", x, dft_hid_im)
        y = torch.einsum("ts,bse->bte", dft_seq_re, a) - torch.einsum("ts,bse->bte", dft_seq_im, b)
        return y

    x = A_x
    g = _sigmoid((x * gate_w).sum(-1, keepdims=True))
    a = _A_ref(x, A_wqkv, A_wo)
    b = _B_ref(x, B_dft_seq_re, B_dft_seq_im, B_dft_hid_re, B_dft_hid_im)
    return g * a + (1 - g) * b
```

```cpp
#include <hip/hip_runtime.h>
#include <hip/hip_bf16.h>
typedef unsigned short u16;
typedef __attribute__((ext_vector_type(8))) short bf16x8;
typedef __attribute__((ext_vector_type(4))) float f32x4;
__device__ __forceinline__ float bf2f(u16 v){ return __uint_as_float(((unsigned)v)<<16); }
__device__ __forceinline__ u16 f2bf(float f){ __hip_bfloat16 b=__float2bfloat16(f); return *(u16*)&b; }
__device__ __forceinline__ float rbf(float f){ return bf2f(f2bf(f)); }

constexpr int S=2048, D=1024;

__global__ void k_gate(const u16* x,const u16* gw,float* g){
  int s=blockIdx.x; int t=threadIdx.x; float a=0;
  for(int d=t;d<D;d+=64) a+=rbf(bf2f(x[s*D+d])*bf2f(gw[d]));
  for(int o=32;o;o>>=1) a+=__shfl_xor(a,o);
  if(t==0){ float z=rbf(a); float gg=rbf(1.f/(1.f+__expf(-z))); g[s]=gg; }
}
template<bool BT,int EPI>
__global__ __launch_bounds__(256) void k_gemm(const u16* A,int lda,const u16* B,int ldb,int K,u16* C,int ldc,const float* g,float sgn,int accum,float* Cf){
  __shared__ u16 sA[64][40], sB[64][40];
  int bm=blockIdx.y*64,bn=blockIdx.x*64; int tid=threadIdx.x,w=tid>>6,l=tid&63; int wr=w>>1,wc=w&1;
  f32x4 acc[2][2]={};
  for(int k0=0;k0<K;k0+=32){
    { int r=tid>>2,c=(tid&3)*8; *(bf16x8*)&sA[r][c]=*(const bf16x8*)&A[(size_t)(bm+r)*lda+k0+c];
      if(BT) *(bf16x8*)&sB[r][c]=*(const bf16x8*)&B[(size_t)(bn+r)*ldb+k0+c];
      else { int kk=tid>>3,n=(tid&7)*8; bf16x8 v=*(const bf16x8*)&B[(size_t)(k0+kk)*ldb+bn+n]; for(int j=0;j<8;j++) sB[n+j][kk]=v[j]; } }
    __syncthreads();
    bf16x8 a[2],b[2];
    for(int i=0;i<2;i++){ a[i]=*(bf16x8*)&sA[wr*32+i*16+(l&15)][(l>>4)*8]; b[i]=*(bf16x8*)&sB[wc*32+i*16+(l&15)][(l>>4)*8]; }
    for(int i=0;i<2;i++)for(int j=0;j<2;j++) acc[i][j]=__builtin_amdgcn_mfma_f32_16x16x32_bf16(a[i],b[j],acc[i][j],0,0,0);
    __syncthreads();
  }
  for(int i=0;i<2;i++)for(int j=0;j<2;j++)for(int r=0;r<4;r++){
    int row=bm+wr*32+i*16+(l>>4)*4+r, col=bn+wc*32+j*16+(l&15); float v=acc[i][j][r];
    if(EPI==0) C[(size_t)row*ldc+col]=f2bf(v);
    if(EPI==1) C[(size_t)row*ldc+col]=f2bf(sgn*(1.f-g[row])*v);
    if(EPI==2){ float p=accum?Cf[(size_t)row*ldc+col]:0.f; Cf[(size_t)row*ldc+col]=p+v; C[(size_t)row*ldc+col]=f2bf(p+v);}
  }
}
__global__ void k_attn(const u16* qkv,const float* g,u16* o,int ldo){
  int s=blockIdx.x; int h=threadIdx.x>>5, d=threadIdx.x&31;
  float q=bf2f(qkv[(size_t)s*3072+h*32+d]); float lg[8]; float mx=-3e38f;
  for(int w=0;w<8;w++){ int t=s-2*w; float v=-1e9f; { int tt=t<0?0:t; float p=q*bf2f(qkv[(size_t)tt*3072+1024+h*32+d]); for(int of=16;of;of>>=1) p+=__shfl_xor(p,of); if(t>=0) v=rbf(rbf(p)*0.17677669529663687f);} lg[w]=v; mx=fmaxf(mx,v);}
  float sum=0; for(int w=0;w<8;w++){ lg[w]=__expf(lg[w]-mx); sum+=lg[w]; }
  float acc=0; for(int w=0;w<8;w++){ int t=s-2*w; int tt=t<0?0:t; acc+=rbf(lg[w]/sum)*bf2f(qkv[(size_t)tt*3072+2048+h*32+d]); }
  o[(size_t)s*ldo+h*32+d]=f2bf(g[s]*rbf(acc));
}
extern "C" void kernel_launch(void* const* d_in,const int* in_sizes,int n_in,void* d_out,int out_size,void* d_ws,size_t ws_size,hipStream_t stream){
  const u16* x=(const u16*)d_in[0]; const u16* wqkv=(const u16*)d_in[1]; const u16* wo=(const u16*)d_in[2];
  const u16* cr=(const u16*)d_in[4]; const u16* ci=(const u16*)d_in[5]; const u16* wre=(const u16*)d_in[6]; const u16* wim=(const u16*)d_in[7]; const u16* gw=(const u16*)d_in[8];
  char* ws=(char*)d_ws; u16* qkv=(u16*)ws; u16* a2=(u16*)(ws+(16<<20)); float* g=(float*)(ws+(32<<20)); float* cf=(float*)(ws+(40<<20));
  u16* out=(u16*)d_out;
  k_gate<<<S,64,0,stream>>>(x,gw,g);
  k_gemm<false,0><<<dim3(3072/64,S/64),256,0,stream>>>(x,D,wqkv,3072,D,qkv,3072,g,1.f,0,nullptr);
  k_attn<<<S,1024,0,stream>>>(qkv,g,a2,3072);
  k_gemm<false,1><<<dim3(D/64,S/64),256,0,stream>>>(cr,S,x,D,S,a2+1024,3072,g,1.f,0,nullptr);
  k_gemm<false,1><<<dim3(D/64,S/64),256,0,stream>>>(ci,S,x,D,S,a2+2048,3072,g,-1.f,0,nullptr);
  k_gemm<false,2><<<dim3(D/64,S/64),256,0,stream>>>(a2,3072,wo,D,D,out,D,g,1.f,0,cf);
  k_gemm<true,2><<<dim3(D/64,S/64),256,0,stream>>>(a2+1024,3072,wre,D,D,out,D,g,1.f,1,cf);
  k_gemm<true,2><<<dim3(D/64,S/64),256,0,stream>>>(a2+2048,3072,wim,D,D,out,D,g,1.f,1,cf);
}
```

```cpp
#include <hip/hip_runtime.h>

typedef unsigned short u16;
typedef unsigned int u32;
typedef __attribute__((ext_vector_type(8))) __bf16 bf16x8;
typedef __attribute__((ext_vector_type(2))) __bf16 bf16x2;
typedef __attribute__((ext_vector_type(4))) float f32x4;
typedef __attribute__((ext_vector_type(2))) float f32x2;
typedef __attribute__((ext_vector_type(4))) u32 u32x4;
typedef __attribute__((ext_vector_type(2))) u32 u32x2;
typedef __attribute__((ext_vector_type(4))) short s16x4;
typedef __amdgpu_buffer_rsrc_t rsrc_t;
#define LAS __attribute__((address_space(3)))
#define DI __device__ __forceinline__

#define ROT1 1
#define ROT2 1
#define PREWARM 1
#define LPRIO 2
#define CPRIO 0
#define PW0 4
#define XROT 1
#define STAUX 0
#define XF32 0

constexpr u32 WS_BH = 0;
constexpr u32 BH_HALF = 64u * 80u * 1024u;
constexpr u32 WS_O = 2u * BH_HALF;
constexpr u32 WS_G = WS_O + 2048u * 4096u;
constexpr u32 WS_FLAG = WS_G + 65536u;
constexpr u32 WS_X = WS_FLAG + 65536u;
constexpr u32 WS_END = WS_X + 256u * 32768u;
constexpr u32 WS_STAMP = WS_END;

DI rsrc_t mk_rsrc(const void* p, u32 bytes) { return __builtin_amdgcn_make_buffer_rsrc((void*)p, 0, bytes, 0x00020000); }
template <int IMM> DI void dma16(rsrc_t r, u32 lds, u32 voff, u32 soff) {
    __builtin_amdgcn_raw_ptr_buffer_load_lds(r, (LAS void*)(size_t)lds, 16, voff, soff, IMM, 0);
}
template <int N> DI void wait_vm() { asm volatile("s_waitcnt vmcnt(%0)" ::"i"(N) : "memory"); }
DI void wait_lgkm0() { __builtin_amdgcn_s_waitcnt(0xC07F); }
DI void bar() { asm volatile("" ::: "memory"); __builtin_amdgcn_s_barrier(); asm volatile("" ::: "memory"); }
DI void bar_unless(int t, int last) { asm volatile("s_cmp_eq_u32 %0, %1\n\ts_cbranch_scc1 1f\n\ts_barrier\n1:" ::"s"(t), "s"(last) : "scc", "memory"); }
#define SCHED() __builtin_amdgcn_sched_barrier(0)
DI bf16x8 lds128(u32 a) { return *(const LAS bf16x8*)(size_t)a; }
DI s16x4 ldstr(u32 a) { return __builtin_amdgcn_ds_read_tr16_b64_v4i16((LAS s16x4*)(size_t)a); }
DI bf16x8 tr_cat(s16x4 lo, s16x4 hi) {
    typedef __attribute__((ext_vector_type(8))) short s16x8;
    s16x8 v = __builtin_shufflevector(lo, hi, 0, 1, 2, 3, 4, 5, 6, 7);
    return __builtin_bit_cast(bf16x8, v);
}
DI f32x4 mfma(bf16x8 a, bf16x8 b, f32x4 c) { return __builtin_amdgcn_mfma_f32_16x16x32_bf16(a, b, c, 0, 0, 0); }
DI void mfma_acc(f32x4& c, bf16x8 a, bf16x8 b) { asm volatile("v_mfma_f32_16x16x32_bf16 %0, %1, %2, %0" : "+v"(c) : "v"(a), "v"(b)); }
DI void mfma_drain() { asm volatile("s_nop 15" ::: "memory"); SCHED(); }
DI u32 pk2(float lo, float hi) { f32x2 v = {lo, hi}; return __builtin_bit_cast(u32, __builtin_convertvector(v, bf16x2)); }
DI u32x2 pk4(f32x4 v) { u32x2 r = {pk2(v[0], v[1]), pk2(v[2], v[3])}; return r; }
DI bf16x8 cat(u32x2 a, u32x2 b) { u32x4 v = {a[0], a[1], b[0], b[1]}; return __builtin_bit_cast(bf16x8, v); }
DI u32x4 widen(u32x2 p0, u32x2 p1) {
    auto x = __builtin_amdgcn_permlane16_swap(p0[0], p1[0], false, false);
    auto y = __builtin_amdgcn_permlane16_swap(p0[1], p1[1], false, false);
    u32x4 r = {x[0], y[0], x[1], y[1]};
    return r;
}
DI float bf2f(u32 v) { return __uint_as_float(v << 16); }
DI float rbf(float f) { return bf2f(pk2(f, 0.f) & 0xffffu); }
DI float red4max(float x) {
    auto a = __builtin_amdgcn_permlane16_swap(__float_as_uint(x), __float_as_uint(x), false, false);
    x = fmaxf(__uint_as_float(a[0]), __uint_as_float(a[1]));
    auto b = __builtin_amdgcn_permlane32_swap(__float_as_uint(x), __float_as_uint(x), false, false);
    return fmaxf(__uint_as_float(b[0]), __uint_as_float(b[1]));
}
DI float red4sum(float x) {
    auto a = __builtin_amdgcn_permlane16_swap(__float_as_uint(x), __float_as_uint(x), false, false);
    x = __uint_as_float(a[0]) + __uint_as_float(a[1]);
    auto b = __builtin_amdgcn_permlane32_swap(__float_as_uint(x), __float_as_uint(x), false, false);
    return __uint_as_float(b[0]) + __uint_as_float(b[1]);
}
template <int MODE> DI int korder(int t, int n, int c) {
    if (MODE == 1) { if ((t & 3) == c) { t += 4; if (t >= n) t -= n; } return t; }
    if (MODE == 2) {
        if (t == 0) return c;
        const int u = t - 1, bb = u >> 2, p = u & 3;
        if (bb != (n >> 2) - 1) return p == c ? 4 * bb + 4 + c : 4 * bb + p;
        return 4 * bb + (p < c ? p : p + 1);
    }
    return t;
}
#define ST_DECL
#define ST(i) do {} while (0)
#define ST_ACC_BEGIN
#define ST_ACC_END(i)
#define ST_OUT(ws, kern) do {} while (0)

constexpr u32 K1_W = 0, K1_WSZ = 2 * 8192;
constexpr u32 K1_A = 2 * K1_WSZ, K1_ASZ = 144 * 128;
constexpr u32 K1_Q = K1_A + 3 * K1_ASZ, K1_QSZ = 3 * 8192;
constexpr u32 K1_LDS = K1_Q + 3 * K1_QSZ;
constexpr int K1_NT = 16;

__global__ __launch_bounds__(512, 2) void k1(const u16* __restrict__ x, const u16* __restrict__ wqkv, const u16* __restrict__ wo,
                                             const u16* __restrict__ cr, const u16* __restrict__ ci, const u16* __restrict__ wre,
                                             const u16* __restrict__ wim, const u16* __restrict__ gw, char* __restrict__ ws) {
    const int b = blockIdx.x, xcd = b & 7, rr = b >> 3;
    const int ti = rr >> 2, tj = rr & 3, xm = xcd >> 2, xn = xcd & 3;
    const int tm = xm * 8 + ti, tn = xn * 4 + tj, s0 = tm * 128;
    const int wid = __builtin_amdgcn_readfirstlane(threadIdx.x >> 6), lane = threadIdx.x & 63;
    ST_DECL
    if (wid >= 4) {
        const int lw = wid - 4;
        __builtin_amdgcn_s_setprio(LPRIO);
                const rsrc_t rx = mk_rsrc(x, 2048u * 2048u), rq = mk_rsrc(wqkv, 1024u * 6144u), rre = mk_rsrc(wre, 1024u * 2048u), rim = mk_rsrc(wim, 1024u * 2048u);
        const u32 l3 = lane >> 3, l7 = lane & 7, l4 = lane >> 4;
        const u32 va0 = l3 * 2048u + ((l7 ^ l4) << 4), va1 = l3 * 2048u + ((l7 ^ l4 ^ 4u) << 4);
        const int hrow = s0 - 16 + 4 * lw + (int)l3;
        const u32 vh = (u32)(hrow < 0 ? 0 : hrow) * 2048u + ((l7 ^ ((2u * lw + l4) & 7u)) << 4);
        const u32 vq0 = l3 * 6144u + ((l7 ^ ((l4 & 1u) << 1)) << 4), vq1 = l3 * 6144u + ((l7 ^ ((l4 & 1u) << 1) ^ 4u) << 4);
        const u32 sa = (u32)(s0 + 32 * lw) * 2048u, sw = (u32)(64 * tn + 16 * lw) * 2048u, sq = (u32)(16 * lw) * 6144u + (u32)tn * 128u;
        const int cls = (ti + tj) & 3, rot = (xn * 4 + xm * 8) & 15;
        auto kof = [&](int t) { return (korder<ROT1>(t, K1_NT, cls) + (XROT ? rot : 0)) & 15; };
        auto ldAQ = [&](int t, int slot) {
            const u32 ks = kof(t), kb = ks * 128u;
            const u32 la = K1_A + slot * K1_ASZ, lq = K1_Q + slot * K1_QSZ + lw * 2048u, q0 = sq + ks * (64u * 6144u);
            if (lane < 32) dma16<0>(rx, la + lw * 512u, vh, kb);
            dma16<0>(rx, la + 2048u + lw * 4096u, va0, sa + kb);
            dma16<0>(rx, la + 2048u + lw * 4096u + 1024u, va1, sa + kb + 16384u);
            dma16<0>(rx, la + 2048u + lw * 4096u + 2048u, va0, sa + kb + 32768u);
            dma16<0>(rx, la + 2048u + lw * 4096u + 3072u, va1, sa + kb + 49152u);
#pragma unroll
            for (int m = 0; m < 3; ++m) {
                dma16<0>(rq, lq + m * 8192u, vq0, q0 + m * 2048u);
                dma16<0>(rq, lq + m * 8192u + 1024u, vq1, q0 + m * 2048u + 8u * 6144u);
            }
        };
        auto ldW = [&](int t, int slot) {
            const u32 kb = kof(t) * 128u, lwb = K1_W + slot * K1_WSZ + lw * 2048u;
            dma16<0>(rre, lwb, va0, sw + kb);
            dma16<0>(rre, lwb + 1024u, va1, sw + kb + 16384u);
            dma16<0>(rim, lwb + 8192u, va0, sw + kb);
            dma16<0>(rim, lwb + 8192u + 1024u, va1, sw + kb + 16384u);
        };
        ldAQ(0, 0); ldW(0, 0); ldAQ(1, 1);
        wait_vm<11>(); bar();
        ST(1);
        ldW(1, 1); ldAQ(2, 2);
        wait_vm<11>(); bar();
        int s3 = 0;
        u32 keep = 0;
        const u16* pw = ((xcd >> 2) ? ci : cr) + (size_t)((xcd & 3) * 512 + rr * 16 + lw * 4 + (lane >> 5)) * 2048 + (lane & 31) * 64;
        const u16* pwo = wo + (size_t)(64 * (b >> 4) + lane) * 1024 + 64 * (b & 15);
        for (int t = 0; t < K1_NT - 3; ++t) {
            { ST_ACC_BEGIN ldW(t + 2, t & 1); ldAQ(t + 3, s3); ST_ACC_END(4) }
            if (PREWARM && t >= PW0 && t < PW0 + 8 && ((t - PW0) & 3) == lw) { asm volatile("global_load_dword %0, %1, off" : "+v"(keep) : "v"(pw)); pw += 2 * 2048; }
            if (t == PW0 - 1 && lw == 3) asm volatile("global_load_dword %0, %1, off" : "+v"(keep) : "v"(pwo));
            s3 = s3 == 2 ? 0 : s3 + 1;
            { ST_ACC_BEGIN wait_vm<11>(); ST_ACC_END(5) }
            { ST_ACC_BEGIN bar(); ST_ACC_END(6) }
        }
        ldW(K1_NT - 1, (K1_NT - 1) & 1);
        wait_vm<0>(); bar();
        ST(2);
        __builtin_amdgcn_s_setprio(0);
        {
            const int eb = 4 * (b & 15) + lw, e = lane & 15, g = lane >> 4;
#pragma unroll
            for (int f = 0; f < 2; ++f) {
                const int kb = 2 * (b >> 4) + f;
                const u16* p = wo + (size_t)(32 * kb + 8 * g) * 1024 + 16 * eb + e;
                u32x4 v;
#pragma unroll
                for (int j = 0; j < 4; ++j) v[j] = (u32)p[(2 * j) * 1024] | ((u32)p[(2 * j + 1) * 1024] << 16);
                *(u32x4*)(ws + WS_BH + (u32)(kb >> 4) * BH_HALF + (u32)(eb * 80 + 64 + (kb & 15)) * 1024u + lane * 16) = v;
            }
        }
        {
            const u32x4 w0 = *(const u32x4*)(gw + lane * 16), w1 = *(const u32x4*)(gw + lane * 16 + 8);
#pragma unroll
            for (int f = 0; f < 2; ++f) {
                const int row = b * 8 + lw * 2 + f;
                const u32x4 x0 = *(const u32x4*)(x + (size_t)row * 1024 + lane * 16), x1 = *(const u32x4*)(x + (size_t)row * 1024 + lane * 16 + 8);
                float a = 0.f;
#pragma unroll
                for (int j = 0; j < 4; ++j) {
                    a += rbf(bf2f(x0[j] & 0xffffu) * bf2f(w0[j] & 0xffffu)) + rbf(bf2f(x0[j] >> 16) * bf2f(w0[j] >> 16));
                    a += rbf(bf2f(x1[j] & 0xffffu) * bf2f(w1[j] & 0xffffu)) + rbf(bf2f(x1[j] >> 16) * bf2f(w1[j] >> 16));
                }
#pragma unroll
                for (int o = 32; o; o >>= 1) a += __shfl_xor(a, o);
                if (lane == 0) ((float*)(ws + WS_G))[row] = rbf(1.f / (1.f + __expf(-rbf(a))));
            }
        }
        if (lw == 0 && lane == 0) *(u32*)(ws + WS_FLAG + b * 128) = 0u;
        asm volatile("" ::"v"(keep));
        ST(3); ST_OUT(ws, 0);
        return;
    }
    __builtin_amdgcn_s_setprio(CPRIO);
    const int hh = wid & 1, mh = wid >> 1;
    const u32 r_ = lane & 15, g_ = lane >> 4, q4 = r_ >> 2, p4 = lane & 3;
    const u32 rs0 = r_ * 128u + ((g_ ^ (r_ >> 1)) << 4), rs1 = rs0 ^ 64u;
    const u32 swb = (((q4 >> 1) & 1u) << 1) | ((g_ & 1u) << 2);
    const u32 tq0 = (8u * g_ + q4) * 128u + (((4u * hh + (p4 >> 1)) ^ swb) << 4) + (p4 & 1u) * 8u, tq1 = tq0 ^ 32u;
    const u32 offA = K1_A + mh * 8192u, offW = K1_W + hh * 4096u;
    f32x4 acc[10][5];
#pragma unroll
    for (int n = 0; n < 10; ++n)
#pragma unroll
        for (int m = 0; m < 5; ++m) acc[n][m] = (f32x4){0.f, 0.f, 0.f, 0.f};
    bf16x8 A[5], B[10];
    s16x4 T[6][2];
#define FQ(n) tr_cat(T[n][0], T[n][1])
#define MM(n, m) do { if ((n) < 4) mfma_acc(acc[n][m], FQ((n) < 6 ? (n) : 0), A[m]); else if ((n) < 6) mfma_acc(acc[n][m], A[m], FQ((n) < 6 ? (n) : 0)); else mfma_acc(acc[n][m], A[m], B[n]); SCHED(); } while (0)
#define RDA(m, base) do { A[m] = lds128((base) + (m) * 2048u); SCHED(); } while (0)
#define RDQ(n, t, h) do { T[n][t] = ldstr((((n) & 1) ? qb1 : qb0) + ((n) >> 1) * 8192u + (h) * 4096u + (t) * 512u); SCHED(); } while (0)
#define RDW(n, base) do { B[n] = lds128((base) + (((n) - 6) >> 1) * 8192u + ((n) & 1) * 2048u); SCHED(); } while (0)
#define UPD(x) do { x; SCHED(); } while (0)
    u32 a0 = offA + rs0, a1 = offA + rs1, qb0 = K1_Q + tq0, qb1 = K1_Q + tq1, w0 = offW + rs0, w1 = offW + rs1;
    wait_lgkm0();
    bar();
    ST(1);
    RDQ(0, 0, 0); RDQ(0, 1, 0); RDA(1, a0); RDQ(1, 0, 0); RDQ(1, 1, 0); RDA(2, a0); RDQ(2, 0, 0); RDQ(2, 1, 0); RDA(3, a0); RDA(4, a0);
    int s3 = 0;
#define HALF(h, ac, wc, an, U1, U2, U3, U4, SYNC)                                                          \
    MM(0, 1); RDA(0, ac); MM(1, 1); MM(2, 1); RDQ(3, 0, h);                                                \
    MM(0, 2); RDQ(3, 1, h); MM(1, 2); MM(2, 2); RDQ(4, 0, h);                                              \
    MM(0, 3); RDQ(4, 1, h); MM(1, 3); MM(2, 3); RDQ(5, 0, h);                                              \
    MM(0, 4); RDQ(5, 1, h); MM(1, 4); MM(2, 4); MM(2, 0); RDW(6, wc);                                      \
    MM(3, 1); UPD(U1); MM(3, 2); RDW(7, wc); MM(3, 3); UPD(U2); MM(3, 4); RDW(8, wc); MM(3, 0); UPD(U3);   \
    MM(4, 1); RDW(9, wc); MM(4, 2); UPD(U4); MM(4, 3); MM(4, 4); MM(4, 0);                                 \
    MM(5, 1); MM(5, 2); MM(5, 3); MM(5, 4); MM(5, 0);                                                      \
    MM(6, 1); MM(6, 2); MM(6, 3); MM(6, 4);                                                                \
    SYNC;                                                                                                  \
    MM(7, 1); RDQ(0, 0, 1 - (h)); MM(8, 1); RDQ(0, 1, 1 - (h)); MM(9, 1); RDA(1, an);                      \
    MM(7, 2); RDQ(1, 0, 1 - (h)); MM(8, 2); RDQ(1, 1, 1 - (h)); MM(9, 2); RDA(2, an);                      \
    MM(7, 3); RDQ(2, 0, 1 - (h)); MM(8, 3); RDQ(2, 1, 1 - (h)); MM(9, 3); RDA(3, an);                      \
    MM(7, 4); MM(8, 4); MM(9, 4); RDA(4, an);
#pragma unroll 1
    for (int t = 0; t < K1_NT; ++t) {
        u32 da, dq;
        HALF(0, a0, w0, a1, da = s3 == 2 ? (u32)(-2 * (int)K1_ASZ) : K1_ASZ, a0 += da, dq = s3 == 2 ? (u32)(-2 * (int)K1_QSZ) : K1_QSZ, (w0 ^= K1_WSZ, s3 = s3 == 2 ? 0 : s3 + 1), (void)0)
        HALF(1, a1, w1, a0, a1 += da, qb0 += dq, qb1 += dq, w1 ^= K1_WSZ, { wait_lgkm0(); SCHED(); ST_ACC_BEGIN bar_unless(t, K1_NT - 1); ST_ACC_END(6) SCHED(); })
    }
    mfma_drain();
    ST(2);
    u32 lz = __builtin_amdgcn_mbcnt_hi(~0u, __builtin_amdgcn_mbcnt_lo(~0u, 0u));
    asm volatile("" : "+v"(lz));
    const u32 r = lz & 15, g = lz >> 4;
    const u32 gp = ((g & 1u) << 1) | (g >> 1);
    const rsrc_t rws = mk_rsrc(ws, WS_G);
    {
        const u32 dst = WS_BH + (u32)((4 * tn + 2 * hh) * 80 + (s0 >> 5) + 2 * mh) * 1024u, dl = (gp * 16u + r) * 16u;
#pragma unroll
        for (int c = 0; c < 2; ++c)
#pragma unroll
            for (int pz = 0; pz < 2; ++pz) {
                __builtin_amdgcn_raw_buffer_store_b128(widen(pk4(acc[6 + c][1 + 2 * pz]), pk4(acc[6 + c][2 + 2 * pz])), rws, dl + pz * 1024u, dst + c * 81920u, STAUX);
                __builtin_amdgcn_raw_buffer_store_b128(widen(pk4(-acc[8 + c][1 + 2 * pz]), pk4(-acc[8 + c][2 + 2 * pz])), rws, dl + pz * 1024u, dst + BH_HALF + c * 81920u, STAUX);
            }
    }
    ST(4);
    bf16x8 Kf[5]; u32x2 Vp[2][5];
#pragma unroll
    for (int m = 0; m < 5; ++m) { Kf[m] = cat(pk4(acc[2][m]), pk4(acc[3][m])); Vp[0][m] = pk4(acc[4][m]); Vp[1][m] = pk4(acc[5][m]); }
    const u32 odst = WS_O + (u32)(s0 + 64 * mh) * 4096u + (u32)(2 * tn + hh) * 64u, ol = r * 4096u + gp * 16u;
    const float sc = 0.17677669529663687f * 1.4426950408889634f, ninf = -__builtin_inff();
    const float edge = -1e9f * 1.4426950408889634f;
    const bool first = (s0 + 64 * mh) == 0;
#pragma unroll
    for (int m = 1; m < 5; ++m) {
        const bf16x8 Qf = cat(pk4(acc[0][m]), pk4(acc[1][m]));
        const f32x4 z = {0.f, 0.f, 0.f, 0.f};
        f32x4 sp = mfma(Kf[m - 1], Qf, z), scur = mfma(Kf[m], Qf, z);
        float mx = ninf;
#pragma unroll
        for (int j = 0; j < 4; ++j) {
            const int dc = (int)r - (int)(4 * g + j), dp = dc + 16;
            float vp = sp[j] * sc; if (m == 1 && first) vp = edge;
            sp[j] = (dp <= 14 && !(dp & 1)) ? vp : ninf;
            scur[j] = (dc >= 0 && dc <= 14 && !(dc & 1)) ? scur[j] * sc : ninf;
            mx = fmaxf(mx, fmaxf(sp[j], scur[j]));
        }
        mx = red4max(mx);
        float sum = 0.f;
#pragma unroll
        for (int j = 0; j < 4; ++j) { sp[j] = __builtin_amdgcn_exp2f(sp[j] - mx); scur[j] = __builtin_amdgcn_exp2f(scur[j] - mx); sum += sp[j] + scur[j]; }
        const float inv = 1.f / red4sum(sum);
        const bf16x8 Pf = cat(pk4(sp * inv), pk4(scur * inv));
        const f32x4 o0 = mfma(cat(Vp[0][m - 1], Vp[0][m]), Pf, z), o1 = mfma(cat(Vp[1][m - 1], Vp[1][m]), Pf, z);
        __builtin_amdgcn_raw_buffer_store_b128(widen(pk4(o0), pk4(o1)), rws, ol, odst + (m - 1) * 65536u, STAUX);
    }
    ST(3); ST_OUT(ws, 0);
#undef MM
#undef RDA
#undef RDQ
#undef RDW
#undef HALF
#undef FQ
#undef UPD
}

constexpr u32 K2_STG = 32768;
constexpr int K2_S = 3, K2_NT = 40;
constexpr u32 K2_LDS = K2_S * K2_STG;

__global__ __launch_bounds__(512, 2) void k2(const u16* __restrict__ cr, const u16* __restrict__ ci, char* __restrict__ ws, u16* __restrict__ out) {
    const int b = blockIdx.x, xcd = b & 7, rr = b >> 3;
    const int hf = xcd >> 2, ti = rr >> 3, tn = rr & 7, tm = (xcd & 3) * 4 + ti;
    const int wid = __builtin_amdgcn_readfirstlane(threadIdx.x >> 6), lane = threadIdx.x & 63;
    ST_DECL
    if (wid >= 4) {
        const int lw = wid - 4;
        __builtin_amdgcn_s_setprio(LPRIO);
                const rsrc_t rc = mk_rsrc(hf ? ci : cr, 2048u * 4096u), ro = mk_rsrc(ws + WS_O, 2048u * 4096u), rb = mk_rsrc(ws + WS_BH + hf * BH_HALF, BH_HALF);
        const u32 l3 = lane >> 3, l7 = lane & 7, l4 = lane >> 4;
        const u32 va0 = l3 * 4096u + ((l7 ^ l4) << 4), va1 = l3 * 4096u + ((l7 ^ l4 ^ 4u) << 4), vb = lane * 16u;
        const u32 sa = (u32)(128 * tm + 32 * lw) * 4096u, sb = (u32)(8 * tn + 2 * lw) * 81920u;
        const int cls = (ti + tn) & 3;
        auto ld = [&](int t, int slot) {
            const u32 la = slot * K2_STG + lw * 4096u, lb = la + 16384u;
            u32 ks;
            if (t < 32) {
                ks = korder<ROT2>(t, 32, cls);
                const u32 s = sa + ks * 128u;
                dma16<0>(rc, la, va0, s); dma16<0>(rc, la + 1024u, va1, s + 32768u); dma16<0>(rc, la + 2048u, va0, s + 65536u); dma16<0>(rc, la + 3072u, va1, s + 98304u);
            } else {
                ks = korder<ROT2>(t - 32, 8, cls);
                const u32 s = sa + hf * 1024u + ks * 128u;
                dma16<0>(ro, la, va0, s); dma16<0>(ro, la + 1024u, va1, s + 32768u); dma16<0>(ro, la + 2048u, va0, s + 65536u); dma16<0>(ro, la + 3072u, va1, s + 98304u);
                ks += 32;
            }
            const u32 s = sb + ks * 2048u;
            dma16<0>(rb, lb, vb, s); dma16<1024>(rb, lb, vb, s);
            dma16<0>(rb, lb + 2048u, vb, s + 81920u); dma16<1024>(rb, lb + 2048u, vb, s + 81920u);
        };
        ld(0, 0); ld(1, 1);
        wait_vm<8>(); bar();
        ST(1);
        int s3 = 2;
        for (int t = 2; t < K2_NT; ++t) {
            { ST_ACC_BEGIN ld(t, s3); ST_ACC_END(4) }
            s3 = s3 == 2 ? 0 : s3 + 1;
            { ST_ACC_BEGIN wait_vm<8>(); ST_ACC_END(5) }
            { ST_ACC_BEGIN bar(); ST_ACC_END(6) }
        }
        wait_vm<0>(); bar();
        ST(2); ST_OUT(ws, 1);
        return;
    }
    __builtin_amdgcn_s_setprio(CPRIO);
    const int wm = wid >> 1, wn = wid & 1;
    const u32 r = lane & 15, g = lane >> 4;
    const u32 ra0 = wm * 8192u + r * 128u + ((g ^ (r >> 1)) << 4), ra1 = ra0 ^ 64u, rbb = 16384u + wn * 8192u + lane * 16u;
    float gate[4];
#pragma unroll
    for (int m = 0; m < 4; ++m) gate[m] = ((const float*)(ws + WS_G))[128 * tm + 64 * wm + 16 * m + r];
    f32x4 acc[4][4], accd[4][4];
#pragma unroll
    for (int n = 0; n < 4; ++n)
#pragma unroll
        for (int m = 0; m < 4; ++m) { acc[n][m] = (f32x4){0.f, 0.f, 0.f, 0.f}; accd[n][m] = acc[n][m]; }
    bf16x8 PA[4], PB[4], QA[4], QB[4];
#define RD(XA, XB, ab, bb) _Pragma("unroll") for (int i = 0; i < 4; ++i) { XB[i] = lds128((bb) + i * 2048u); XA[i] = lds128((ab) + i * 2048u); }
#define MMS(XA, XB) _Pragma("unroll") for (int n = 0; n < 4; ++n) _Pragma("unroll") for (int m = 0; m < 4; ++m) acc[n][m] = mfma(XB[n], XA[m], acc[n][m]);
#define ILV() _Pragma("unroll") for (int i = 0; i < 8; ++i) { __builtin_amdgcn_sched_group_barrier(0x8, 1, 0); __builtin_amdgcn_sched_group_barrier(0x100, 1, 0); } __builtin_amdgcn_sched_group_barrier(0x8, 8, 0);
    asm volatile("" : "+v"(gate[0]), "+v"(gate[1]), "+v"(gate[2]), "+v"(gate[3]));
    wait_lgkm0();
    bar();
    ST(1);
    RD(PA, PB, ra0, rbb)
    u32 cur = 0;
    for (int t = 0; t < K2_NT; ++t) {
        const u32 nxt = cur == 2 * K2_STG ? 0 : cur + K2_STG;
        SCHED();
        RD(QA, QB, cur + ra1, cur + rbb + 1024u)
        MMS(PA, PB)
        ILV()
        SCHED();
        if (t < K2_NT - 1) { wait_lgkm0(); SCHED(); ST_ACC_BEGIN bar(); ST_ACC_END(6) }
        SCHED();
        RD(PA, PB, nxt + ra0, nxt + rbb)
        MMS(QA, QB)
        ILV()
        SCHED();
        if (t == 31) {
#pragma unroll
            for (int n = 0; n < 4; ++n)
#pragma unroll
                for (int m = 0; m < 4; ++m) { accd[n][m] = acc[n][m] * (1.f - gate[m]); acc[n][m] = (f32x4){0.f, 0.f, 0.f, 0.f}; }
        }
        cur = nxt;
    }
    ST(2);
#pragma unroll
    for (int n = 0; n < 4; ++n)
#pragma unroll
        for (int m = 0; m < 4; ++m) acc[n][m] = acc[n][m] * gate[m] + accd[n][m];
    const rsrc_t rxs = mk_rsrc(ws + WS_X, 256u * 32768u);
    const u32 mine = (u32)b * 32768u + wid * 8192u + lane * 16u, theirs = (u32)(b ^ 4) * 32768u + wid * 8192u + lane * 16u;
    f32x4 keepv[4][2];
    if (hf == 0) {
#pragma unroll
        for (int n = 0; n < 4; ++n) { keepv[n][0] = acc[n][0]; keepv[n][1] = acc[n][1]; acc[n][0] = acc[n][2]; acc[n][1] = acc[n][3]; }
    } else {
#pragma unroll
        for (int n = 0; n < 4; ++n) { keepv[n][0] = acc[n][2]; keepv[n][1] = acc[n][3]; }
    }
#pragma unroll
    for (int n = 0; n < 4; ++n) {
        if (XF32) {
            __builtin_amdgcn_raw_buffer_store_b128(__builtin_bit_cast(u32x4, acc[n][0]), rxs, mine + n * 2048u, 0, 16);
            __builtin_amdgcn_raw_buffer_store_b128(__builtin_bit_cast(u32x4, acc[n][1]), rxs, mine + n * 2048u + 1024u, 0, 16);
        } else {
            const u32x2 p0 = pk4(acc[n][0]), p1 = pk4(acc[n][1]);
            const u32x4 v = {p0[0], p0[1], p1[0], p1[1]};
            __builtin_amdgcn_raw_buffer_store_b128(v, rxs, mine + n * 1024u, 0, 16);
        }
    }
    wait_vm<0>();
    ST(3);
    bar();
    if (wid == 0) {
        if (lane == 0) __hip_atomic_store((u32*)(ws + WS_FLAG + b * 128), 1u, __ATOMIC_RELAXED, __HIP_MEMORY_SCOPE_AGENT);
        const u32* pf = (const u32*)(ws + WS_FLAG + (b ^ 4) * 128);
        for (u32 spin = 0; spin < (1u << 22); ++spin) {
            if (__builtin_amdgcn_readfirstlane(__hip_atomic_load(pf, __ATOMIC_RELAXED, __HIP_MEMORY_SCOPE_AGENT)) != 0u) break;
            __builtin_amdgcn_s_sleep(1);
        }
            }
    bar();
    ST(4);
#pragma unroll
    for (int n = 0; n < 4; ++n) {
        if (XF32) {
            keepv[n][0] += __builtin_bit_cast(f32x4, __builtin_amdgcn_raw_buffer_load_b128(rxs, theirs + n * 2048u, 0, 16));
            keepv[n][1] += __builtin_bit_cast(f32x4, __builtin_amdgcn_raw_buffer_load_b128(rxs, theirs + n * 2048u + 1024u, 0, 16));
        } else {
            const u32x4 v = __builtin_amdgcn_raw_buffer_load_b128(rxs, theirs + n * 1024u, 0, 16);
            keepv[n][0] += (f32x4){bf2f(v[0] & 0xffffu), __uint_as_float(v[0] & 0xffff0000u), bf2f(v[1] & 0xffffu), __uint_as_float(v[1] & 0xffff0000u)};
            keepv[n][1] += (f32x4){bf2f(v[2] & 0xffffu), __uint_as_float(v[2] & 0xffff0000u), bf2f(v[3] & 0xffffu), __uint_as_float(v[3] & 0xffff0000u)};
        }
    }
    const u32 gp = ((g & 1u) << 1) | (g >> 1);
    u16* op = out + (size_t)(128 * tm + 64 * wm + 32 * hf + r) * 1024 + 128 * tn + 64 * wn + gp * 8;
#pragma unroll
    for (int m = 0; m < 2; ++m)
#pragma unroll
        for (int pz = 0; pz < 2; ++pz)
            *(u32x4*)(op + m * 16384 + pz * 32) = widen(pk4(keepv[2 * pz][m]), pk4(keepv[2 * pz + 1][m]));
    ST(5); ST_OUT(ws, 1);
#undef RD
#undef MMS
#undef ILV
}

extern "C" void kernel_launch(void* const* d_in, const int* in_sizes, int n_in, void* d_out, int out_size, void* d_ws, size_t ws_size, hipStream_t stream) {
    const u16* x = (const u16*)d_in[0]; const u16* wqkv = (const u16*)d_in[1]; const u16* wo = (const u16*)d_in[2];
    const u16* cr = (const u16*)d_in[4]; const u16* ci = (const u16*)d_in[5]; const u16* wre = (const u16*)d_in[6];
    const u16* wim = (const u16*)d_in[7]; const u16* gw = (const u16*)d_in[8];
    (void)hipFuncSetAttribute((const void*)k1, hipFuncAttributeMaxDynamicSharedMemorySize, (int)K1_LDS);
    (void)hipFuncSetAttribute((const void*)k2, hipFuncAttributeMaxDynamicSharedMemorySize, (int)K2_LDS);
    k1<<<256, 512, K1_LDS, stream>>>(x, wqkv, wo, cr, ci, wre, wim, gw, (char*)d_ws);
    k2<<<256, 512, K2_LDS, stream>>>(cr, ci, (char*)d_ws, (u16*)d_out);
}
```

```cpp
#include <hip/hip_runtime.h>

typedef unsigned short u16;
typedef unsigned int u32;
typedef unsigned long long u64;
typedef __attribute__((ext_vector_type(8))) __bf16 bf16x8;
typedef __attribute__((ext_vector_type(2))) __bf16 bf16x2;
typedef __attribute__((ext_vector_type(4))) float f32x4;
typedef __attribute__((ext_vector_type(2))) float f32x2;
typedef __attribute__((ext_vector_type(4))) u32 u32x4;
typedef __attribute__((ext_vector_type(2))) u32 u32x2;
typedef __attribute__((ext_vector_type(4))) short s16x4;
typedef __amdgpu_buffer_rsrc_t rsrc_t;
#define LAS __attribute__((address_space(3)))
#define GAS __attribute__((address_space(1)))
#define DI __device__ __forceinline__

#define ROT1 1
#define ROT2 1
#define PREWARM 0
#define XROT 1
#define LATEPOLL 4u
#define TOUCH1 0
#define DYN 0
#define SPLITB 2
#define PUBU 0
#define TLBW 0
#define PW0 4
#define EPAD 2048u
#define OPAD 2048u
#define T3A 0
#define T3B 2
#define TJS 0
#define ILV 1
#define NOSTP 0
#define TA 3
#define LPRIO 0

constexpr u32 WS_BH = 0;
constexpr u32 EBS = 81920u + EPAD, OTS = 32768u + OPAD;
constexpr u32 BH_HALF = 64u * EBS;
constexpr u32 WS_O = 2u * BH_HALF;
constexpr u32 WS_G = WS_O + 128u * OTS;
constexpr u32 WS_X = WS_G + 65536u;
constexpr u32 WS_FLAG = WS_X + 256u * 16384u;
constexpr u32 F_AB = WS_FLAG, F_O = F_AB + 32768u, F_X = F_O + 32768u, F_EPOCH = F_X + 4u * 32768u;
constexpr u32 WS_END = F_EPOCH + 128u;
constexpr u32 WS_STAMP = WS_END;
constexpr u32 SC1 = 16;
constexpr u32 SPIN_MAX = 1u << 20;

DI rsrc_t mk_rsrc(const void* p, u32 bytes) { return __builtin_amdgcn_make_buffer_rsrc((void*)p, 0, bytes, 0x00020000); }
template <int IMM, int AUX> DI void dma16(rsrc_t r, u32 lds, u32 voff, u32 soff) {
    __builtin_amdgcn_raw_ptr_buffer_load_lds(r, (LAS void*)(size_t)lds, 16, voff, soff, IMM, AUX);
}
template <int N> DI void wait_vm() { asm volatile("s_waitcnt vmcnt(%0)" ::"i"(N) : "memory"); }
DI void wait_lgkm0() { __builtin_amdgcn_s_waitcnt(0xC07F); }
DI void bar() { asm volatile("" ::: "memory"); __builtin_amdgcn_s_barrier(); asm volatile("" ::: "memory"); }
DI void bar_unless(int t, int last) { asm volatile("s_cmp_eq_u32 %0, %1\n\ts_cbranch_scc1 1f\n\ts_barrier\n1:" ::"s"(t), "s"(last) : "scc", "memory"); }
#define SCHED() __builtin_amdgcn_sched_barrier(0)
DI bf16x8 lds128(u32 a) { return *(const LAS bf16x8*)(size_t)a; }
DI s16x4 ldstr(u32 a) { return __builtin_amdgcn_ds_read_tr16_b64_v4i16((LAS s16x4*)(size_t)a); }
DI bf16x8 tr_cat(s16x4 lo, s16x4 hi) {
    typedef __attribute__((ext_vector_type(8))) short s16x8;
    s16x8 v = __builtin_shufflevector(lo, hi, 0, 1, 2, 3, 4, 5, 6, 7);
    return __builtin_bit_cast(bf16x8, v);
}
DI f32x4 mfma(bf16x8 a, bf16x8 b, f32x4 c) { return __builtin_amdgcn_mfma_f32_16x16x32_bf16(a, b, c, 0, 0, 0); }
DI void mfma_acc(f32x4& c, bf16x8 a, bf16x8 b) { asm volatile("v_mfma_f32_16x16x32_bf16 %0, %1, %2, %0" : "+v"(c) : "v"(a), "v"(b)); }
DI void mfma_drain() { SCHED(); asm volatile("s_nop 15" ::: "memory"); SCHED(); }
DI u32 pk2(float lo, float hi) { f32x2 v = {lo, hi}; return __builtin_bit_cast(u32, __builtin_convertvector(v, bf16x2)); }
DI u32x2 pk4(f32x4 v) { u32x2 r = {pk2(v[0], v[1]), pk2(v[2], v[3])}; return r; }
DI bf16x8 cat(u32x2 a, u32x2 b) { u32x4 v = {a[0], a[1], b[0], b[1]}; return __builtin_bit_cast(bf16x8, v); }
DI u32x4 widen(u32x2 p0, u32x2 p1) {
    auto x = __builtin_amdgcn_permlane16_swap(p0[0], p1[0], false, false);
    auto y = __builtin_amdgcn_permlane16_swap(p0[1], p1[1], false, false);
    u32x4 r = {x[0], y[0], x[1], y[1]};
    return r;
}
DI float dot2(u32 a, u32 b, float c) { return __builtin_amdgcn_fdot2_f32_bf16(__builtin_bit_cast(bf16x2, a), __builtin_bit_cast(bf16x2, b), c, false); }
DI float bf2f(u32 v) { return __uint_as_float(v << 16); }
DI float rbf(float f) { return bf2f(pk2(f, 0.f) & 0xffffu); }
DI float fmax2(float a, float b) { return __builtin_amdgcn_fmed3f(a, b, __builtin_inff()); }
DI float red4max(float x) {
    auto a = __builtin_amdgcn_permlane16_swap(__float_as_uint(x), __float_as_uint(x), false, false);
    x = fmax2(__uint_as_float(a[0]), __uint_as_float(a[1]));
    auto b = __builtin_amdgcn_permlane32_swap(__float_as_uint(x), __float_as_uint(x), false, false);
    return fmax2(__uint_as_float(b[0]), __uint_as_float(b[1]));
}
DI float red4sum(float x) {
    auto a = __builtin_amdgcn_permlane16_swap(__float_as_uint(x), __float_as_uint(x), false, false);
    x = __uint_as_float(a[0]) + __uint_as_float(a[1]);
    auto b = __builtin_amdgcn_permlane32_swap(__float_as_uint(x), __float_as_uint(x), false, false);
    return __uint_as_float(b[0]) + __uint_as_float(b[1]);
}
DI u32 lane_id() { u32 l = __builtin_amdgcn_mbcnt_hi(~0u, __builtin_amdgcn_mbcnt_lo(~0u, 0u)); asm volatile("" : "+v"(l)); return l; }
template <int MODE> DI int korder(int t, int n, int c) {
    if (MODE == 1) { if ((t & 3) == c) { t += 4; if (t >= n) t -= n; } return t; }
    return t;
}
DI void poll_ptr(const char* p, u64 tag) {
    const GAS u64* q = (const GAS u64*)p;
#pragma unroll 1
    for (u32 spin = 0; spin < SPIN_MAX; ++spin) {
        const u64 v = __hip_atomic_load(q, __ATOMIC_RELAXED, __HIP_MEMORY_SCOPE_AGENT);
        if (__all(v == tag)) break;
        __builtin_amdgcn_s_sleep(2);
    }
    asm volatile("" ::: "memory");
}
DI void poll_words(const char* p, u32 n, u64 tag, u32 lane) { poll_ptr(p + (lane < n ? lane : 0u) * 128u, tag); }
DI void publish(u32 lds_cnt, u32 last, char* flag, u64 tag, u32 lane) {
    wait_vm<0>();
    if (lane == 0) {
        const u32 old = __hip_atomic_fetch_add((LAS u32*)(size_t)lds_cnt, 1u, __ATOMIC_RELAXED, __HIP_MEMORY_SCOPE_WORKGROUP);
        if (old == last) __hip_atomic_store((GAS u64*)flag, tag, __ATOMIC_RELAXED, __HIP_MEMORY_SCOPE_AGENT);
    }
    asm volatile("" ::: "memory");
}
#define ST_DECL
#define STP(i) do {} while (0)
#define STP_OUT(ws) do {} while (0)
#define ST(i) do {} while (0)
#define ST_OUT(ws) do {} while (0)

constexpr u32 K1_W = 0, K1_WSZ = 2 * 8192;
constexpr u32 K1_A = 2 * K1_WSZ, K1_ASZ = 144 * 128;
constexpr u32 K1_Q = K1_A + 3 * K1_ASZ, K1_QSZ = 3 * 8192;
constexpr u32 CTL = K1_Q + 3 * K1_QSZ;
constexpr u32 LDS_BYTES = CTL + 64 + 512;
DI void touch_sc1(const char* p) { __builtin_amdgcn_global_load_lds((const u32*)p, (LAS u32*)(size_t)(CTL + 64), 4, 0, SC1); }
DI void touch(const u16* p) { __builtin_amdgcn_global_load_lds((const u32*)p, (LAS u32*)(size_t)(CTL + 64), 4, 0, 0); }
constexpr int K1_NT = 16;
constexpr u32 K2_A0 = K1_W, K2_A1 = K1_A + K1_ASZ, K2_A2 = K1_A + 2 * K1_ASZ, K2_B0 = K1_W + K1_WSZ, K2_B1 = K1_Q + K1_QSZ, K2_B2 = K1_Q + 2 * K1_QSZ;
constexpr int K2_ND = 32, K2_NT = 40;

__global__ __launch_bounds__(512, 2) void fused(const u16* x, const u16* wqkv, const u16* wo,
                                                const u16* cr, const u16* ci, const u16* wre,
                                                const u16* wim, const u16* gw, char* ws, u16* out) {
    const int b = blockIdx.x, xcd = b & 7, rr = b >> 3;
    const int ti = rr >> 2, tj = (rr + TJS) & 3, xm = xcd >> 2, xn = xcd & 3;
    const int tm = xm * 8 + ti, tn = xn * 4 + tj, s0 = tm * 128;
    const int hf = xcd >> 2, ui = rr >> 3, un = rr & 7, um = (xcd & 3) * 4 + ui;
    const int wid = __builtin_amdgcn_readfirstlane(threadIdx.x >> 6);
    ST_DECL
    asm volatile("" : "+s"(x), "+s"(wqkv), "+s"(wo), "+s"(cr), "+s"(ci), "+s"(wre), "+s"(wim), "+s"(gw), "+s"(ws), "+s"(out));
    if (__builtin_expect(wid >= 4, 1)) {
        const int lw = wid - 4;
        const u32 lane = threadIdx.x & 63;
        __builtin_amdgcn_s_setprio(LPRIO);
        u32 stp_ = 0; (void)stp_;
        const u32 l3 = lane >> 3, l7 = lane & 7, l4 = lane >> 4;
        const rsrc_t rws = mk_rsrc(ws, WS_X);
        auto ldA2 = [&](int t, u32 la) {
            la += lw * 4096u;
            const int cls = (ui + un) & 3;
            if (t < K2_ND) {
                const rsrc_t rc = mk_rsrc(hf ? ci : cr, 2048u * 4096u);
                const u32 va0 = l3 * 4096u + ((l7 ^ l4) << 4), va1 = va0 ^ 64u;
                const u32 s = (u32)(128 * um + 32 * lw) * 4096u + korder<ROT2>(t, K2_ND, cls) * 128u;
                dma16<0, 0>(rc, la, va0, s); dma16<0, 0>(rc, la + 1024u, va1, s + 32768u); dma16<0, 0>(rc, la + 2048u, va0, s + 65536u); dma16<0, 0>(rc, la + 3072u, va1, s + 98304u);
            } else {
                const u32 s = WS_O + (u32)(8 * um + 2 * lw) * OTS + hf * 16384u + korder<ROT2>(t - K2_ND, 8, cls) * 2048u;
                dma16<0, SC1>(rws, la, lane * 16u, s); dma16<1024, SC1>(rws, la, lane * 16u, s);
                dma16<0, SC1>(rws, la + 2048u, lane * 16u, s + OTS); dma16<1024, SC1>(rws, la + 2048u, lane * 16u, s + OTS);
            }
        };
        auto ldB2 = [&](int t, u32 lb) {
            const int cls = (ui + un) & 3;
            const u32 ks = t < K2_ND ? korder<ROT2>(t, K2_ND, cls) : K2_ND + korder<ROT2>(t - K2_ND, 8, cls);
            const u32 s = WS_BH + hf * BH_HALF + (u32)(8 * un + 2 * lw) * EBS + ks * 2048u;
            lb += lw * 4096u;
            dma16<0, SC1>(rws, lb, lane * 16u, s); dma16<1024, SC1>(rws, lb, lane * 16u, s);
            dma16<0, SC1>(rws, lb + 2048u, lane * 16u, s + EBS); dma16<1024, SC1>(rws, lb + 2048u, lane * 16u, s + EBS);
        };
        {
            const rsrc_t rx = mk_rsrc(x, 2048u * 2048u), rq = mk_rsrc(wqkv, 1024u * 6144u), rre = mk_rsrc(wre, 1024u * 2048u), rim = mk_rsrc(wim, 1024u * 2048u);
            const u32 va0 = l3 * 2048u + ((l7 ^ l4) << 4), va1 = va0 ^ 64u;
            const int hrow = s0 - 16 + 4 * lw + (int)l3;
            const u32 vh = (u32)(hrow < 0 ? 0 : hrow) * 2048u + ((l7 ^ ((2u * lw + l4) & 7u)) << 4);
            const u32 vq0 = l3 * 6144u + ((l7 ^ ((l4 & 1u) << 1)) << 4), vq1 = vq0 ^ 64u;
            const u32 sa = (u32)(s0 + 32 * lw) * 2048u, sw = (u32)(64 * tn + 16 * lw) * 2048u, sq = (u32)(16 * lw) * 6144u + (u32)tn * 128u;
            const int cls = (ti + tj) & 3, rot = XROT == 1 ? (xn * 4 + xm * 8) & 15 : XROT == 2 ? ((xn ^ xm) & 1) * 8 : 0;
            auto kof = [&](int t) { return (korder<TOUCH1 == 1 ? 0 : ROT1>(t, K1_NT, cls) + rot) & 15; };
            auto ldX = [&](int t, int slot) {
                const u32 kb = kof(t) * 128u, la = K1_A + slot * K1_ASZ;
                if (lane < 32) dma16<0, 0>(rx, la + lw * 512u, vh, kb);
                dma16<0, 0>(rx, la + 2048u + lw * 4096u, va0, sa + kb);
                dma16<0, 0>(rx, la + 2048u + lw * 4096u + 1024u, va1, sa + kb + 16384u);
                dma16<0, 0>(rx, la + 2048u + lw * 4096u + 2048u, va0, sa + kb + 32768u);
                dma16<0, 0>(rx, la + 2048u + lw * 4096u + 3072u, va1, sa + kb + 49152u);
            };
            auto ldQ = [&](int t, int slot) {
                const u32 lq = K1_Q + slot * K1_QSZ + lw * 2048u, q0 = sq + kof(t) * (64u * 6144u);
#pragma unroll
                for (int m = 0; m < 3; ++m) {
                    dma16<0, 0>(rq, lq + m * 8192u, vq0, q0 + m * 2048u);
                    dma16<0, 0>(rq, lq + m * 8192u + 1024u, vq1, q0 + m * 2048u + 8u * 6144u);
                }
            };
            auto ldAQ = [&](int t, int slot) {
                if (!ILV) { ldX(t, slot); ldQ(t, slot); return; }
                const u32 ks = kof(t), kb = ks * 128u, la = K1_A + slot * K1_ASZ, lx = la + 2048u + lw * 4096u;
                const u32 lq = K1_Q + slot * K1_QSZ + lw * 2048u, q0 = sq + ks * (64u * 6144u);
                dma16<0, 0>(rq, lq, vq0, q0);
                dma16<0, 0>(rx, lx, va0, sa + kb);
                dma16<0, 0>(rq, lq + 1024u, vq1, q0 + 8u * 6144u);
                dma16<0, 0>(rx, lx + 1024u, va1, sa + kb + 16384u);
                dma16<0, 0>(rq, lq + 8192u, vq0, q0 + 2048u);
                dma16<0, 0>(rx, lx + 2048u, va0, sa + kb + 32768u);
                dma16<0, 0>(rq, lq + 8192u + 1024u, vq1, q0 + 2048u + 8u * 6144u);
                dma16<0, 0>(rx, lx + 3072u, va1, sa + kb + 49152u);
                dma16<0, 0>(rq, lq + 16384u, vq0, q0 + 4096u);
                if (lane < 32) dma16<0, 0>(rx, la + lw * 512u, vh, kb);
                dma16<0, 0>(rq, lq + 16384u + 1024u, vq1, q0 + 4096u + 8u * 6144u);
            };
            auto ldW = [&](int t, int slot) {
                const u32 kb = kof(t) * 128u, lwb = K1_W + slot * K1_WSZ + lw * 2048u;
                dma16<0, 0>(rre, lwb, va0, sw + kb);
                dma16<0, 0>(rre, lwb + 1024u, va1, sw + kb + 16384u);
                dma16<0, 0>(rim, lwb + 8192u, va0, sw + kb);
                dma16<0, 0>(rim, lwb + 8192u + 1024u, va1, sw + kb + 16384u);
            };
            const u16* pg; u32 gstep;
            if (lw < 2) { pg = x + (size_t)(xm * 1024 + rr * 32 + lw * 16 + (lane >> 2)) * 1024 + (lane & 3) * 64; gstep = 256; }
            else if (lw == 2) { pg = ((rr & 16) ? wim : wre) + (size_t)(xn * 256 + (rr & 15) * 16 + (lane >> 2)) * 1024 + (lane & 3) * 64; gstep = 256; }
            else if (lane < 48) { pg = wqkv + (size_t)(rr * 8 + lane / 12u) * 3072 + ((lane % 12u) >> 2) * 1024 + xn * 256 + (lane & 3) * 64; gstep = 256u * 3072u; }
            else { pg = x + (size_t)(xm * 1008 + (rr & 1) * 8 + ((lane - 48) >> 2)) * 1024 + (lane & 3) * 64; gstep = 256; }
            if (TOUCH1 == 3) {
                if (lw & 1) { pg = wqkv + (size_t)b * 3072 + (lane < 48 ? lane : 0u) * 64; gstep = 256u * 3072u; }
                else { pg = (lane < 32 ? x + (size_t)(b * 8 + (lane >> 2)) * 1024 : ((b & 128) ? wim : wre) + (size_t)((b & 127) * 8 + ((lane - 32) >> 2)) * 1024) + (lane & 3) * 64; gstep = 256; }
            }
            const u32 g2nd = lane < 48 ? 4u * 3072u : 4u * 1024u;
            auto touch_group = [&](int gq) {
                const u16* p = pg + (size_t)(((gq + (rot >> 2)) & 3) * gstep);
                touch(p);
                if (TOUCH1 != 3 && lw == 3) touch(p + g2nd);
            };
            if (TOUCH1 == 3) touch_group(lw >> 1); else if (TOUCH1) touch_group(0);
            ldAQ(0, 0); ldW(0, 0); ldAQ(1, 1);
            const u16* pw = (hf ? ci : cr) + (size_t)((xcd & 3) * 512 + rr * 16 + lw * 4 + l4) * 2048 + (lane & 15) * 64;
            const u16* pt = lw == 3 ? wo + (size_t)(64 * tm + lane) * 1024 + 64 * tn : lw == 2 ? gw + (lane & 15) * 64 : x + (size_t)(s0 + 8 * tn + 4 * lw + l4) * 1024 + (lane & 15) * 64;
            const int kbw = 2 * tm + (lw >> 1), grow = s0 + 8 * tn + 2 * lw;
            const u16* pwo = wo + (size_t)(32 * kbw + 8 * l4 + 2) * 1024 + 64 * tn + 32 * (lw & 1) + 2 * (lane & 15);
            const u16* pwo2 = pwo + 4096;
            const u16* pgw = gw + lane * 16;
            const u16* pgx = x + (size_t)grow * 1024 + lane * 16;
            u32 fo = (l4 * 16u + 2u * l7 + (l3 & 1u)) * 16u;
            const u32 tl = lw * 64u + lane;
            const char* ptl = tl < 96u ? ws + (tl * 262144u < WS_END - 128u ? tl * 262144u : WS_END - 128u) : (const char*)out + (tl & 15u) * 262144u;
            int ta = TA;
            asm volatile("" : "+s"(ta), "+v"(pw), "+v"(pt), "+v"(pwo), "+v"(pwo2), "+v"(pgw), "+v"(pgx), "+v"(fo), "+v"(ptl));
            wait_vm<11>(); bar();
            ST(1); STP(0);
            ldW(1, 1); ldAQ(2, 2);
            if (TOUCH1 == 1) touch_group(1);
            int s3 = 0;
            u32 kw = kof(2), kq = kof(3);
            u32 pW = K1_W + lw * 2048u, pX = K1_A, pQ = K1_Q + lw * 2048u, oW = sw + kw * 128u, oX = kq * 128u, oQ = sq + kq * (64u * 6144u);
            asm volatile("" : "+s"(pW), "+s"(pX), "+s"(pQ), "+s"(oW), "+s"(oX), "+s"(oQ));
#pragma unroll 1
            for (int t = 0; t < K1_NT - 4; ++t) {
                wait_vm<11>(); bar();
                SCHED();
                dma16<0, 0>(rre, pW, va0, oW);
                dma16<0, 0>(rre, pW + 1024u, va1, oW + 16384u);
                dma16<0, 0>(rim, pW + 8192u, va0, oW);
                dma16<0, 0>(rim, pW + 8192u + 1024u, va1, oW + 16384u);
                {
                    const u32 lx = pX + 2048u + lw * 4096u, sx = sa + oX;
                    dma16<0, 0>(rq, pQ, vq0, oQ);
                    dma16<0, 0>(rx, lx, va0, sx);
                    dma16<0, 0>(rq, pQ + 1024u, vq1, oQ + 8u * 6144u);
                    dma16<0, 0>(rx, lx + 1024u, va1, sx + 16384u);
                    dma16<0, 0>(rq, pQ + 8192u, vq0, oQ + 2048u);
                    dma16<0, 0>(rx, lx + 2048u, va0, sx + 32768u);
                    dma16<0, 0>(rq, pQ + 8192u + 1024u, vq1, oQ + 2048u + 8u * 6144u);
                    dma16<0, 0>(rx, lx + 3072u, va1, sx + 49152u);
                    dma16<0, 0>(rq, pQ + 16384u, vq0, oQ + 4096u);
                    dma16<0, 0>(rq, pQ + 16384u + 1024u, vq1, oQ + 4096u + 8u * 6144u);
                    if (lane < 32) dma16<0, 0>(rx, pX + lw * 512u, vh, oX);
                }
                SCHED();
                STP(t + 1);
                if (TOUCH1 == 1) {
                    const int ph = (t + (lw < 2 ? 0 : SPLITB)) & 3;
                    if (ph == cls) { if (t < 8) touch_group(2 + (t >> 2)); else if (PREWARM && t < 12) { touch(pw); touch(pw + 1024); } }
                } else if (PREWARM == 1) {
                    if (t >= 4 && t < 12 && ((t + 3) & 3) == cls) touch(pw + (t < 8 ? 0 : 1024));
                } else if (PREWARM == 3) {
                    if (t >= PW0 && t < PW0 + 8 && ((t + cls) & 3) == lw) touch(pw + (t < PW0 + 4 ? 0 : 1024));
                } else if (PREWARM == 4) {
                    const int ph = (t + 3 - cls) & 3;
                    if (t >= PW0 && t < PW0 + 8 && (ph == 1 || ph == 2) && (lw >> 1) == (ph & 1)) touch(pw + (t < PW0 + 4 ? 0 : 1024));
                }
                if (TOUCH1 == 3 && t == T3A + (lw >> 1) * T3B) touch_group(2 + (lw >> 1));
                if (t == ta) touch(pt);
                if (TLBW && t == ta - 1 && lw < 2) touch_sc1(ptl);
                s3 = s3 == 2 ? 0 : s3 + 1;
                kw = kq; kq = kof(t + 4);
                pW ^= K1_WSZ; pX = K1_A + s3 * K1_ASZ; pQ = K1_Q + lw * 2048u + s3 * K1_QSZ;
                oW = sw + kw * 128u; oX = kq * 128u; oQ = sq + kq * (64u * 6144u);
                asm volatile("" : "+s"(pW), "+s"(pX), "+s"(pQ), "+s"(oW), "+s"(oX), "+s"(oQ));
            }
            wait_vm<11>(); bar();
            STP(13);
            ldW(14, 0); ldX(15, 0);
            wait_vm<5>(); bar();
            STP(14);
            ldW(15, 1); ldQ(15, 0);
            wait_vm<6>(); bar();
            ldA2(0, K2_A0);
            u32 wv[8];
            u32x4 gv[6];
                    asm volatile("global_load_dword %0, %8, off offset:-4096\n\tglobal_load_dword %1, %8, off offset:-2048\n\t"
                                 "global_load_dword %2, %8, off\n\tglobal_load_dword %3, %8, off offset:2048\n\t"
                                 "global_load_dword %4, %9, off offset:-4096\n\tglobal_load_dword %5, %9, off offset:-2048\n\t"
                                 "global_load_dword %6, %9, off\n\tglobal_load_dword %7, %9, off offset:2048"
                                 : "=&v"(wv[0]), "=&v"(wv[1]), "=&v"(wv[2]), "=&v"(wv[3]), "=&v"(wv[4]), "=&v"(wv[5]), "=&v"(wv[6]), "=&v"(wv[7])
                                 : "v"(pwo), "v"(pwo2) : "memory");
                    asm volatile("global_load_dwordx4 %0, %6, off\n\tglobal_load_dwordx4 %1, %6, off offset:16\n\t"
                                 "global_load_dwordx4 %2, %7, off\n\tglobal_load_dwordx4 %3, %7, off offset:16\n\t"
                                 "global_load_dwordx4 %4, %7, off offset:2048\n\tglobal_load_dwordx4 %5, %7, off offset:2064"
                                 : "=&v"(gv[0]), "=&v"(gv[1]), "=&v"(gv[2]), "=&v"(gv[3]), "=&v"(gv[4]), "=&v"(gv[5]) : "v"(pgw), "v"(pgx) : "memory");
            bar();
            STP(15);
            asm volatile("s_waitcnt vmcnt(0)" : "+v"(wv[0]), "+v"(wv[1]), "+v"(wv[2]), "+v"(wv[3]), "+v"(wv[4]), "+v"(wv[5]), "+v"(wv[6]), "+v"(wv[7]),
                         "+v"(gv[0]), "+v"(gv[1]), "+v"(gv[2]), "+v"(gv[3]), "+v"(gv[4]), "+v"(gv[5]) :: "memory");
            {
                u32x4 c0, c1;
                const bool up = lane & 8u;
#pragma unroll
                for (int j = 0; j < 4; ++j) {
                    const u32 lo = __builtin_amdgcn_perm(wv[2 * j + 1], wv[2 * j], 0x05040100u), hi = __builtin_amdgcn_perm(wv[2 * j + 1], wv[2 * j], 0x07060302u);
                    const u32 rv = __builtin_amdgcn_update_dpp(0u, up ? lo : hi, 0x128, 0xf, 0xf, false);
                    c0[j] = up ? rv : lo; c1[j] = up ? hi : rv;
                }
                const u32 fs = WS_BH + (u32)(kbw >> 4) * BH_HALF + (u32)(4 * tn + 2 * (lw & 1)) * EBS + (u32)(64 + (kbw & 15)) * 1024u;
                __builtin_amdgcn_raw_buffer_store_b128(c0, rws, fo, fs, SC1);
                __builtin_amdgcn_raw_buffer_store_b128(c1, rws, fo, fs + EBS, SC1);
                    float a0 = 0.f, a1 = 0.f;
#pragma unroll
                    for (int j = 0; j < 4; ++j) {
                        a0 = dot2(gv[2][j], gv[0][j], a0);
                        a0 = dot2(gv[3][j], gv[1][j], a0);
                        a1 = dot2(gv[4][j], gv[0][j], a1);
                        a1 = dot2(gv[5][j], gv[1][j], a1);
                    }
                    a0 = red4sum(a0); a1 = red4sum(a1);
                    float a = (lane & 1) ? a1 : a0, o = (lane & 1) ? a0 : a1;
                    a += __shfl_xor(o, 1);
                    a += __shfl_xor(a, 2); a += __shfl_xor(a, 4); a += __shfl_xor(a, 8);
                    const float gt = rbf(1.f / (1.f + __expf(-rbf(a))));
                    __builtin_amdgcn_raw_buffer_store_b32(__float_as_uint(gt), rws, lane < 2 ? lane * 4u : 0x80000000u, WS_G + grow * 4u, SC1);
            }
            bar();
        }
        ST(2);
        const u64 tag = *(const LAS u64*)(size_t)(CTL + 16);
        ldA2(1, K2_A1);
        bar();
        ldA2(2, K2_A2);
        publish(CTL + 4, 7u, ws + F_O + (tm * 16 + tn) * 128, tag, lane);
        volatile LAS u32* ready = (volatile LAS u32*)(size_t)(CTL + 8);
        if (lw == 0) {
#pragma unroll 1
            for (u32 spin = 0; spin < SPIN_MAX * 8u && ready[-2] < LATEPOLL; ++spin) __builtin_amdgcn_s_sleep(1);
            ST(6);
            poll_words(ws + F_AB + un * 4096, 32, tag, lane);
            if (lane == 0) *ready = 1u;
        } else {
#pragma unroll 1
            for (u32 spin = 0; spin < SPIN_MAX * 8u && *ready == 0u; ++spin) __builtin_amdgcn_s_sleep(1);
        }
        asm volatile("" ::: "memory");
        ST(3);
        ldB2(0, K2_B0); ldB2(1, K2_B1);
        wait_vm<4>(); bar();
        ST(4); STP(16);
        ldB2(2, K2_B2);
        {
            const int cls = (ui + un) & 3;
            const rsrc_t rc = mk_rsrc(hf ? ci : cr, 2048u * 4096u);
            const u32 va0 = l3 * 4096u + ((l7 ^ l4) << 4), va1 = va0 ^ 64u, vb = lane * 16u;
            const u32 sA0 = (u32)(128 * um + 32 * lw) * 4096u, sB0 = WS_BH + hf * BH_HALF + (u32)(8 * un + 2 * lw) * EBS;
            const u32 sO0 = WS_O + (u32)(8 * um + 2 * lw) * OTS + hf * 16384u;
            const char* pfo = ws + F_O + ((lane & 16u) ? (8 * hf + ((lane & 15u) >> 1)) * 16 + 2 * un + (lane & 1u) : um * 16 + (lane & 15u)) * 128;
            u32 lA = K2_A0 + lw * 4096u, lA1 = K2_A1 + lw * 4096u, lA2 = K2_A2 + lw * 4096u, lB = K2_B0 + lw * 4096u, lB1 = K2_B1 + lw * 4096u, lB2 = K2_B2 + lw * 4096u;
            u32 ks = korder<ROT2>(3, K2_ND, cls), sA = sA0 + ks * 128u, sB = sB0 + ks * 2048u;
            int flagt = lw == 0 ? K2_ND - 3 : -1;
            asm volatile("" : "+s"(sA), "+s"(sB), "+s"(lA), "+s"(lB), "+s"(flagt), "+v"(pfo));
            wait_vm<4>();
#pragma unroll 1
            for (int t = 3; t < K2_ND; ++t) {
                wait_vm<8>(); bar();
                SCHED();
                dma16<0, SC1>(rws, lB, vb, sB); dma16<1024, SC1>(rws, lB, vb, sB);
                dma16<0, 0>(rc, lA, va0, sA); dma16<0, 0>(rc, lA + 1024u, va1, sA + 32768u);
                dma16<0, SC1>(rws, lB + 2048u, vb, sB + EBS); dma16<1024, SC1>(rws, lB + 2048u, vb, sB + EBS);
                dma16<0, 0>(rc, lA + 2048u, va0, sA + 65536u); dma16<0, 0>(rc, lA + 3072u, va1, sA + 98304u);
                SCHED();
                { const u32 a = lA, c = lB; lA = lA1; lA1 = lA2; lA2 = a; lB = lB1; lB1 = lB2; lB2 = c; }
                ks = korder<ROT2>(t + 1, K2_ND, cls); sA = sA0 + ks * 128u; sB = sB0 + ks * 2048u;
                if (t == flagt && lane < 32) __builtin_amdgcn_global_load_lds((const u32*)pfo, (LAS u32*)(size_t)(CTL + 64), 16, 0, SC1);
                if (t == flagt + 2 && !__all(*(const volatile LAS u64*)(size_t)(CTL + 64 + (lane & 31u) * 16u) == tag)) poll_ptr(pfo, tag);
                if (t == K2_ND - 1) { ks = korder<ROT2>(0, 8, cls); sA = sO0 + ks * 2048u; sB = sB0 + (K2_ND + ks) * 2048u; }
                asm volatile("" : "+s"(sA), "+s"(sB), "+s"(lA), "+s"(lB));
                STP(t + 14);
            }
#pragma unroll 1
            for (int t = 0; t < 8; ++t) {
                wait_vm<8>(); bar();
                SCHED();
                dma16<0, SC1>(rws, lB, vb, sB); dma16<1024, SC1>(rws, lB, vb, sB);
                dma16<0, SC1>(rws, lA, vb, sA); dma16<1024, SC1>(rws, lA, vb, sA);
                dma16<0, SC1>(rws, lB + 2048u, vb, sB + EBS); dma16<1024, SC1>(rws, lB + 2048u, vb, sB + EBS);
                dma16<0, SC1>(rws, lA + 2048u, vb, sA + OTS); dma16<1024, SC1>(rws, lA + 2048u, vb, sA + OTS);
                SCHED();
                { const u32 a = lA, c = lB; lA = lA1; lA1 = lA2; lA2 = a; lB = lB1; lB1 = lB2; lB2 = c; }
                ks = korder<ROT2>(t + 1, 8, cls); sA = sO0 + ks * 2048u; sB = sB0 + (K2_ND + ks) * 2048u;
                asm volatile("" : "+s"(sA), "+s"(sB), "+s"(lA), "+s"(lB));
                STP(t + 46);
            }
            wait_vm<8>(); bar();
            STP(54);
        }
        wait_vm<0>(); bar();
        ST(5); STP(55); STP_OUT(ws);
        if (b == 0 && lw == 0) {
#pragma unroll 1
            for (int i = 0; i < 4; ++i) poll_words(ws + F_AB + i * 8192, 64, tag, lane);
            if (lane == 0) __hip_atomic_store((GAS u64*)(ws + F_EPOCH), tag, __ATOMIC_RELAXED, __HIP_MEMORY_SCOPE_AGENT);
        }
        ST_OUT(ws);
        return;
    }
    const u64 ep = __hip_atomic_load((const GAS u64*)(ws + F_EPOCH), __ATOMIC_RELAXED, __HIP_MEMORY_SCOPE_AGENT);
    u64 tag = (((u64)__builtin_amdgcn_readfirstlane((u32)(ep >> 32)) << 32) | (u32)__builtin_amdgcn_readfirstlane((u32)ep)) * 6364136223846793005ull + 1442695040888963407ull;
    if (threadIdx.x < 4) ((LAS u32*)(size_t)CTL)[threadIdx.x] = 0u;
    if (threadIdx.x == 4) *(LAS u64*)(size_t)(CTL + 16) = tag;
    const rsrc_t rws = mk_rsrc(ws, WS_FLAG);
    {
        const int hh = wid & 1, mh = wid >> 1;
        f32x4 acc[10][5];
        {
            const u32 lane = threadIdx.x & 63;
            const u32 r_ = lane & 15, g_ = lane >> 4, q4 = r_ >> 2, p4 = lane & 3;
            const u32 rs0 = r_ * 128u + ((g_ ^ (r_ >> 1)) << 4), rs1 = rs0 ^ 64u;
            const u32 swb = (((q4 >> 1) & 1u) << 1) | ((g_ & 1u) << 2);
            const u32 tq0 = (8u * g_ + q4) * 128u + (((4u * hh + (p4 >> 1)) ^ swb) << 4) + (p4 & 1u) * 8u, tq1 = tq0 ^ 32u;
            const u32 offA = K1_A + mh * 8192u, offW = K1_W + hh * 4096u;
#pragma unroll
            for (int n = 0; n < 10; ++n)
#pragma unroll
                for (int m = 0; m < 5; ++m) if (m || (n >= 2 && n < 6)) { acc[n][m] = (f32x4){0.f, 0.f, 0.f, 0.f}; asm volatile("" : "+v"(acc[n][m])); }
            bf16x8 A[5], B[10];
            s16x4 T[6][2];
#define FQ(n) tr_cat(T[n][0], T[n][1])
#define MM(n, m) do { if ((n) < 4) mfma_acc(acc[n][m], FQ((n) < 6 ? (n) : 0), A[m]); else if ((n) < 6) mfma_acc(acc[n][m], A[m], FQ((n) < 6 ? (n) : 0)); else mfma_acc(acc[n][m], A[m], B[n]); SCHED(); } while (0)
#define RDA(m, base) do { A[m] = lds128((base) + (m) * 2048u); SCHED(); } while (0)
#define RDQ(n, t, h) do { T[n][t] = ldstr((((n) & 1) ? qb1 : qb0) + ((n) >> 1) * 8192u + (h) * 4096u + (t) * 512u); SCHED(); } while (0)
#define RDW(n, base) do { B[n] = lds128((base) + (((n) - 6) >> 1) * 8192u + ((n) & 1) * 2048u); SCHED(); } while (0)
#define UPD(x) do { x; SCHED(); } while (0)
            u32 a0 = offA + rs0, a1 = offA + rs1, qb0 = K1_Q + tq0, qb1 = K1_Q + tq1, w0 = offW + rs0, w1 = offW + rs1;
            asm volatile("" : "+s"(tag), "+v"(a0), "+v"(a1), "+v"(qb0), "+v"(qb1), "+v"(w0), "+v"(w1));
            wait_lgkm0();
            bar();
            ST(1);
            RDQ(0, 0, 0); RDQ(0, 1, 0); RDA(1, a0); RDQ(1, 0, 0); RDQ(1, 1, 0); RDA(2, a0); RDQ(2, 0, 0); RDQ(2, 1, 0); RDA(3, a0); RDA(4, a0);
            int s3 = 0;
#define HALF(h, ac, wc, an, U1, U2, U3, U4, SYNC)                                                          \
    MM(0, 1); RDA(0, ac); MM(1, 1); MM(2, 1); RDQ(3, 0, h);                                                \
    MM(0, 2); RDQ(3, 1, h); MM(1, 2); MM(2, 2); RDQ(4, 0, h);                                              \
    MM(0, 3); RDQ(4, 1, h); MM(1, 3); MM(2, 3); RDQ(5, 0, h);                                              \
    MM(0, 4); RDQ(5, 1, h); MM(1, 4); MM(2, 4); MM(2, 0); RDW(6, wc);                                      \
    MM(3, 1); UPD(U1); MM(3, 2); RDW(7, wc); MM(3, 3); UPD(U2); MM(3, 4); RDW(8, wc); MM(3, 0); UPD(U3);   \
    MM(4, 1); RDW(9, wc); MM(4, 2); UPD(U4); MM(4, 3); MM(4, 4); MM(4, 0);                                 \
    MM(5, 1); MM(5, 2); MM(5, 3); MM(5, 4); MM(5, 0);                                                      \
    MM(6, 1); MM(6, 2); MM(6, 3); MM(6, 4);                                                                \
    SYNC;                                                                                                  \
    MM(7, 1); RDQ(0, 0, 1 - (h)); MM(8, 1); RDQ(0, 1, 1 - (h)); MM(9, 1); RDA(1, an);                      \
    MM(7, 2); RDQ(1, 0, 1 - (h)); MM(8, 2); RDQ(1, 1, 1 - (h)); MM(9, 2); RDA(2, an);                      \
    MM(7, 3); RDQ(2, 0, 1 - (h)); MM(8, 3); RDQ(2, 1, 1 - (h)); MM(9, 3); RDA(3, an);                      \
    MM(7, 4); MM(8, 4); MM(9, 4); RDA(4, an);
#pragma unroll 1
            for (int t = 0; t < K1_NT - 3; ++t) {
                u32 da, dq;
                HALF(0, a0, w0, a1, da = s3 == 2 ? (u32)(-2 * (int)K1_ASZ) : K1_ASZ, a0 += da, dq = s3 == 2 ? (u32)(-2 * (int)K1_QSZ) : K1_QSZ, (w0 ^= K1_WSZ, s3 = s3 == 2 ? 0 : s3 + 1), (void)0)
                HALF(1, a1, w1, a0, a1 += da, qb0 += dq, qb1 += dq, w1 ^= K1_WSZ, { wait_lgkm0(); SCHED(); bar(); SCHED(); })
            }
            bf16x8 Y[5];
#define MA(n, m, X) do { mfma_acc(acc[n][m], X[m], B[n]); SCHED(); } while (0)
#define RDY(m, base) do { Y[m] = lds128((base) + (m) * 2048u); SCHED(); } while (0)
            RDW(6, w0); RDW(7, w0);
#pragma unroll 1
            for (int u = 0; u < 3; ++u) {
                u32 da;
                MA(6, 1, A); RDW(8, w0); MA(6, 2, A); RDW(9, w0); MA(7, 1, A); RDY(1, a1); MA(7, 2, A); RDY(2, a1);
                MA(6, 3, A); RDY(3, a1); MA(7, 3, A); RDY(4, a1); MA(6, 4, A); UPD(da = s3 == 2 ? (u32)(-2 * (int)K1_ASZ) : K1_ASZ); MA(7, 4, A); RDW(6, w1);
                MA(8, 1, A); RDW(7, w1); MA(8, 2, A); UPD(a0 += da); MA(9, 1, A); UPD(a1 += da); MA(9, 2, A); UPD(w0 ^= K1_WSZ);
                MA(8, 3, A); UPD(s3 = s3 == 2 ? 0 : s3 + 1); MA(9, 3, A); MA(8, 4, A); MA(9, 4, A); RDW(8, w1);
                MA(6, 1, Y); RDW(9, w1); MA(6, 2, Y); UPD(w1 ^= K1_WSZ); MA(7, 1, Y); MA(7, 2, Y); MA(6, 3, Y); MA(7, 3, Y); MA(6, 4, Y); MA(7, 4, Y);
                wait_lgkm0(); SCHED(); bar(); SCHED();
                MA(8, 1, Y); RDW(6, w0); MA(8, 2, Y); RDA(1, a0); MA(9, 1, Y); RDA(2, a0); MA(9, 2, Y); RDW(7, w0);
                MA(8, 3, Y); RDA(3, a0); MA(9, 3, Y); RDA(4, a0); MA(8, 4, Y); MA(9, 4, Y);
            }
            RDQ(0, 0, 0); RDQ(0, 1, 0); RDQ(1, 0, 0); RDQ(1, 1, 0); RDQ(2, 0, 0); RDQ(2, 1, 0);
            mfma_drain();
            ST(2);
            {
                const u32 ln = lane_id(), r = ln & 15, g = ln >> 4, gp = ((g & 1u) << 1) | (g >> 1);
                const u32 dst = WS_BH + (u32)(4 * tn + 2 * hh) * EBS + (u32)((s0 >> 5) + 2 * mh) * 1024u, dl = (gp * 16u + r) * 16u;
#pragma unroll
                for (int c = 0; c < 4; ++c)
#pragma unroll
                    for (int pz = 0; pz < 2; ++pz)
                        __builtin_amdgcn_raw_buffer_store_b128(widen(pk4(acc[6 + c][1 + 2 * pz]), pk4(acc[6 + c][2 + 2 * pz])), rws, dl + pz * 1024u,
                                                               dst + (c >> 1) * BH_HALF + (c & 1) * EBS, SC1);
            }
            SCHED();
            ST(3);
#define HALFQ(h, ac, an, U1, U2, U3, SYNC)                                                                 \
    MM(0, 1); RDA(0, ac); MM(1, 1); RDQ(3, 0, h); MM(2, 1); RDQ(3, 1, h);                                  \
    MM(0, 2); RDQ(4, 0, h); MM(1, 2); RDQ(4, 1, h); MM(2, 2); RDQ(5, 0, h);                                \
    MM(0, 3); RDQ(5, 1, h); MM(1, 3); UPD(U1); MM(2, 3); UPD(U2);                                          \
    MM(0, 4); UPD(U3); MM(1, 4); MM(2, 4); MM(2, 0); MM(3, 0); MM(4, 0); MM(5, 0);                         \
    SYNC;                                                                                                  \
    MM(3, 1); RDQ(0, 0, 1 - (h)); MM(4, 1); RDQ(0, 1, 1 - (h)); MM(5, 1); RDA(1, an);                      \
    MM(3, 2); RDQ(1, 0, 1 - (h)); MM(4, 2); RDQ(1, 1, 1 - (h)); MM(5, 2); RDA(2, an);                      \
    MM(3, 3); RDQ(2, 0, 1 - (h)); MM(4, 3); RDQ(2, 1, 1 - (h)); MM(5, 3); RDA(3, an);                      \
    MM(3, 4); MM(4, 4); MM(5, 4); RDA(4, an);
            int u = 0, uend = PUBU + 1;
            asm volatile("" : "+s"(uend));
#pragma unroll 1
            for (;;) {
#pragma unroll 1
                do {
                    u32 da, dq;
                    HALFQ(0, a0, a1, da = s3 == 2 ? (u32)(-2 * (int)K1_ASZ) : K1_ASZ, a0 += da, (dq = s3 == 2 ? (u32)(-2 * (int)K1_QSZ) : K1_QSZ, s3 = s3 == 2 ? 0 : s3 + 1), (void)0)
                    HALFQ(1, a1, a0, a1 += da, qb0 += dq, qb1 += dq, { wait_lgkm0(); SCHED(); bar_unless(u, 2); SCHED(); })
                } while (++u != uend);
                if (u == 3) break;
                publish(CTL, 3u, ws + F_AB + (tn * 16 + tm) * 128, tag, lane_id());
                uend = 3;
            }
            mfma_drain();
#undef FQ
#undef MM
#undef MA
#undef RDA
#undef RDY
#undef RDQ
#undef RDW
#undef HALF
#undef HALFQ
        }
        ST(4);
        const u32 lane = lane_id(), r = lane & 15, g = lane >> 4, gp = ((g & 1u) << 1) | (g >> 1);
        bf16x8 Kf[5]; u32x2 Vp[2][5];
#pragma unroll
        for (int m = 0; m < 5; ++m) { Kf[m] = cat(pk4(acc[2][m]), pk4(acc[3][m])); Vp[0][m] = pk4(acc[4][m]); Vp[1][m] = pk4(acc[5][m]); }
        const float sc = 0.17677669529663687f * 1.4426950408889634f, edge = -1e9f / 0.17677669529663687f;
        const bool first = (s0 + 64 * mh) == 0, par = r & 1u;
        const bool cur0 = r >= 4u * g + (r & 1u), cur1 = r >= 4u * g + 2u + (r & 1u);
        const u32 sh = (r & 1u) * 16u;
        const f32x4 z = {0.f, 0.f, 0.f, 0.f};
#pragma unroll
        for (int m = 1; m < 5; ++m) {
            const bf16x8 Qf = cat(pk4(acc[0][m]), pk4(acc[1][m]));
            const f32x4 sp = mfma(Kf[m - 1], Qf, z), sn = mfma(Kf[m], Qf, z);
            float p0 = par ? sp[1] : sp[0], p1 = par ? sp[3] : sp[2];
            if (m == 1) { p0 = first ? edge : p0; p1 = first ? edge : p1; }
            const float v0 = cur0 ? (par ? sn[1] : sn[0]) : p0, v1 = cur1 ? (par ? sn[3] : sn[2]) : p1;
            const float mx = red4max(fmax2(v0, v1));
            const float e0 = __builtin_amdgcn_exp2f((v0 - mx) * sc), e1 = __builtin_amdgcn_exp2f((v1 - mx) * sc);
            const float inv = __builtin_amdgcn_rcpf(red4sum(e0 + e1));
            const u32 pk = pk2(e0 * inv, e1 * inv), w0 = (pk & 0xffffu) << sh, w1 = (pk >> 16) << sh;
            const u32x4 pf = {cur0 ? 0u : w0, cur1 ? 0u : w1, cur0 ? w0 : 0u, cur1 ? w1 : 0u};
            const bf16x8 Pf = __builtin_bit_cast(bf16x8, pf);
            const f32x4 o0 = mfma(cat(Vp[0][m - 1], Vp[0][m]), Pf, z), o1 = mfma(cat(Vp[1][m - 1], Vp[1][m]), Pf, z);
            __builtin_amdgcn_raw_buffer_store_b128(widen(pk4(o0), pk4(o1)), rws, (gp * 16u + r) * 16u, WS_O + (u32)(8 * tm + 4 * mh + m - 1) * OTS + (u32)(2 * tn + hh) * 1024u, SC1);
        }
        ST(5);
    }
    const int wm = wid >> 1, wn = wid & 1;
    f32x4 acc[4][4], accd[4][4];
    u32 gate[4];
    {
        const u32 lane = lane_id(), r = lane & 15, g = lane >> 4;
        u32 ra = wm * 8192u + r * 128u + ((g ^ (r >> 1)) << 4);
        const u32 ra2 = wm * 8192u + lane * 16u, rb = wn * 8192u + lane * 16u;
#pragma unroll
        for (int n = 0; n < 4; ++n)
#pragma unroll
            for (int m = 0; m < 4; ++m) { acc[n][m] = (f32x4){0.f, 0.f, 0.f, 0.f}; accd[n][m] = acc[n][m]; asm volatile("" : "+v"(acc[n][m]), "+v"(accd[n][m])); }
        bf16x8 PA[4], PB[4], QA[4], QB[4];
        u32 pa = ra + K2_A0, pb = rb + K2_B0;
        int s3 = 0, tl = K2_ND - 1;
        asm volatile("" : "+s"(tl));
#define M2(C, n, m, XA, XB) do { mfma_acc(C[n][m], XB[n], XA[m]); SCHED(); } while (0)
#define RA(X, i, base) do { X[i] = lds128((base) + (i) * 2048u); SCHED(); } while (0)
#define HALF2(C, XA, XB, YA, YB, ya, yb, U1, U2, U3)                                                                       \
    M2(C, 0, 0, XA, XB); RA(YB, 0, yb); M2(C, 0, 1, XA, XB); RA(YA, 0, ya); M2(C, 1, 0, XA, XB); RA(YA, 1, ya);            \
    M2(C, 1, 1, XA, XB); RA(YB, 1, yb); M2(C, 0, 2, XA, XB); RA(YA, 2, ya); M2(C, 1, 2, XA, XB); RA(YB, 2, yb);            \
    M2(C, 2, 0, XA, XB); RA(YA, 3, ya); M2(C, 2, 1, XA, XB); RA(YB, 3, yb); M2(C, 2, 2, XA, XB); UPD(U1);                  \
    M2(C, 0, 3, XA, XB); UPD(U2); M2(C, 1, 3, XA, XB); UPD(U3); M2(C, 2, 3, XA, XB);                                       \
    M2(C, 3, 0, XA, XB); M2(C, 3, 1, XA, XB); M2(C, 3, 2, XA, XB); M2(C, 3, 3, XA, XB);
#define STEP2(C, PAH, RANEXT, SYNC)                                                                                                     \
    HALF2(C, PA, PB, QA, QB, PAH, pb + 1024u, s3 = s3 == 2 ? 0 : s3 + 1, pa = (RANEXT) + (s3 == 0 ? K2_A0 : s3 == 1 ? K2_A1 : K2_A2), pb = rb + (s3 == 0 ? K2_B0 : s3 == 1 ? K2_B1 : K2_B2)) \
    wait_lgkm0(); SCHED(); SYNC; SCHED();                                                                                  \
    HALF2(C, QA, QB, PA, PB, pa, pb, (void)0, (void)0, (void)0)
        wait_lgkm0();
        bar();
        ST(6);
        RA(PB, 0, pb); RA(PA, 0, pa); RA(PA, 1, pa); RA(PB, 1, pb); RA(PA, 2, pa); RA(PB, 2, pb); RA(PA, 3, pa); RA(PB, 3, pb);
        {
            int t = 0, tend = 2;
            asm volatile("" : "+s"(tend));
#pragma unroll 1
            for (;;) {
#pragma unroll 1
                do { STEP2(accd, pa ^ 64u, t == tl ? ra2 : ra, bar()) } while (++t != tend);
                if (t == K2_ND) break;
                publish(CTL + 4, 7u, ws + F_O + (tm * 16 + tn) * 128, tag, lane);
                tend = K2_ND;
            }
        }
#pragma unroll
        for (int m = 0; m < 4; ++m) gate[m] = __builtin_amdgcn_raw_buffer_load_b32(rws, r * 4u, WS_G + (u32)(128 * um + 64 * wm + 16 * m) * 4u, SC1);
        SCHED();
#pragma unroll 1
        for (int t = K2_ND; t < K2_NT; ++t) { STEP2(acc, pa + 1024u, ra2, bar_unless(t, K2_NT - 1)) }
        mfma_drain();
#undef M2
#undef RA
#undef HALF2
#undef STEP2
#undef UPD
    }
    ST(7);
    const u32 lane = lane_id(), r = lane & 15, g = lane >> 4;
#pragma unroll
    for (int m = 0; m < 4; ++m) {
        const float gt = __uint_as_float(gate[m]);
        const float gd = hf ? gt - 1.f : 1.f - gt;
#pragma unroll
        for (int n = 0; n < 4; ++n) acc[n][m] = acc[n][m] * gt + accd[n][m] * gd;
    }
    const u32 xl = wid * 4096u + lane * 16u;
    f32x4 keepv[4][2];
#pragma unroll
    for (int n = 0; n < 4; ++n) {
        const u32x2 p0 = pk4(hf ? acc[n][0] : acc[n][2]), p1 = pk4(hf ? acc[n][1] : acc[n][3]);
        const u32x4 v = {p0[0], p0[1], p1[0], p1[1]};
        __builtin_amdgcn_raw_buffer_store_b128(v, rws, xl + n * 1024u, WS_X + (u32)b * 16384u, SC1);
        keepv[n][0] = hf ? acc[n][2] : acc[n][0]; keepv[n][1] = hf ? acc[n][3] : acc[n][1];
    }
    wait_vm<0>();
    if (lane == 0) __hip_atomic_store((GAS u64*)(ws + F_X + (b * 4 + wid) * 128), tag, __ATOMIC_RELAXED, __HIP_MEMORY_SCOPE_AGENT);
    ST(8);
    poll_words(ws + F_X + ((b ^ 4) * 4 + wid) * 128, 1, tag, lane);
    ST(9);
#pragma unroll
    for (int n = 0; n < 4; ++n) {
        const u32x4 v = __builtin_amdgcn_raw_buffer_load_b128(rws, xl + n * 1024u, WS_X + (u32)(b ^ 4) * 16384u, SC1);
        keepv[n][0] += (f32x4){bf2f(v[0] & 0xffffu), __uint_as_float(v[0] & 0xffff0000u), bf2f(v[1] & 0xffffu), __uint_as_float(v[1] & 0xffff0000u)};
        keepv[n][1] += (f32x4){bf2f(v[2] & 0xffffu), __uint_as_float(v[2] & 0xffff0000u), bf2f(v[3] & 0xffffu), __uint_as_float(v[3] & 0xffff0000u)};
    }
    const u32 gp = ((g & 1u) << 1) | (g >> 1);
    u16* op = out + (size_t)(128 * um + 64 * wm + 32 * hf + r) * 1024 + 128 * un + 64 * wn + gp * 8;
#pragma unroll
    for (int m = 0; m < 2; ++m)
#pragma unroll
        for (int pz = 0; pz < 2; ++pz)
            *(GAS u32x4*)(op + m * 16384 + pz * 32) = widen(pk4(keepv[2 * pz][m]), pk4(keepv[2 * pz + 1][m]));
    ST(10);
    ST_OUT(ws);
}

extern "C" void kernel_launch(void* const* d_in, const int* in_sizes, int n_in, void* d_out, int out_size, void* d_ws, size_t ws_size, hipStream_t stream) {
    const u16* x = (const u16*)d_in[0]; const u16* wqkv = (const u16*)d_in[1]; const u16* wo = (const u16*)d_in[2];
    const u16* cr = (const u16*)d_in[4]; const u16* ci = (const u16*)d_in[5]; const u16* wre = (const u16*)d_in[6];
    const u16* wim = (const u16*)d_in[7]; const u16* gw = (const u16*)d_in[8];
    (void)hipFuncSetAttribute((const void*)fused, hipFuncAttributeMaxDynamicSharedMemorySize, (int)LDS_BYTES);
    fused<<<256, 512, LDS_BYTES, stream>>>(x, wqkv, wo, cr, ci, wre, wim, gw, (char*)d_ws, (u16*)d_out);
}
```

```cpp
#include <hip/hip_runtime.h>
#include <type_traits>

typedef unsigned short u16;
typedef unsigned int u32;
typedef unsigned long long u64;
typedef __attribute__((ext_vector_type(8))) __bf16 bf16x8;
typedef __attribute__((ext_vector_type(2))) __bf16 bf16x2;
typedef __attribute__((ext_vector_type(4))) float f32x4;
typedef __attribute__((ext_vector_type(2))) float f32x2;
typedef __attribute__((ext_vector_type(4))) u32 u32x4;
typedef __attribute__((ext_vector_type(2))) u32 u32x2;
typedef __attribute__((ext_vector_type(4))) short s16x4;
typedef __amdgpu_buffer_rsrc_t rsrc_t;
#define LAS __attribute__((address_space(3)))
#define GAS __attribute__((address_space(1)))
#define DI __device__ __forceinline__

constexpr u32 WS_BH = 0;
constexpr u32 EBS = 65536u + 2048u, OTS = 32768u + 2048u;
constexpr u32 BH_HALF = 64u * EBS;
constexpr u32 WS_O = 2u * BH_HALF;
constexpr u32 WS_G = WS_O + 128u * OTS;
constexpr u32 WS_H = WS_G + 65536u;
constexpr u32 WS_X = WS_H + 512u * 2048u;
constexpr u32 WS_FLAG = WS_X + 256u * 16384u;
constexpr u32 F_AB = WS_FLAG, F_O = F_AB + 8u * 32768u, F_X = F_O + 32768u, F_H = F_X + 4u * 32768u, F_EPOCH = F_H + 32768u;
constexpr u32 WS_END = F_EPOCH + 128u;
constexpr u32 SC1 = 16;
constexpr u32 SPIN_MAX = 1u << 20;

DI rsrc_t mk_rsrc(const void* p, u32 bytes) { return __builtin_amdgcn_make_buffer_rsrc((void*)p, 0, bytes, 0x00020000); }
template <int IMM> DI void dma16(rsrc_t r, u32 lds, u32 voff, u32 soff) {
    __builtin_amdgcn_raw_ptr_buffer_load_lds(r, (LAS void*)(size_t)lds, 16, voff, soff, IMM, SC1);
}
template <int N> DI void wait_vm() { asm volatile("s_waitcnt vmcnt(%0)" ::"i"(N) : "memory"); }
DI void wait_lgkm0() { __builtin_amdgcn_s_waitcnt(0xC07F); }
DI void bar() { asm volatile("" ::: "memory"); __builtin_amdgcn_s_barrier(); asm volatile("" ::: "memory"); }
DI void bar_unless(int t, int last) { asm volatile("s_cmp_eq_u32 %0, %1\n\ts_cbranch_scc1 1f\n\ts_barrier\n1:" ::"s"(t), "s"(last) : "scc", "memory"); }
DI void store_word_if(int t, int at, u32 zero, u32x2 v, const char* p) {
    asm volatile("s_cmp_lg_u32 %0, %1\n\ts_cbranch_scc1 1f\n\tglobal_store_dwordx2 %2, %3, %4 sc1\n1:" ::"s"(t), "s"(at), "v"(zero), "v"(v), "s"(p) : "scc", "memory");
}
DI void note_if_drained(u32& done, u32 lds, u32 one) {
    u32 c;
    asm volatile("s_getreg_b32 %0, hwreg(HW_REG_IB_STS, 0, 4)\n\ts_or_b32 %0, %0, %1\n\ts_cbranch_scc1 1f\n\ts_waitcnt vmcnt(0)\n\tds_write_b32 %2, %3 offset:16\n\ts_mov_b32 %1, 1\n1:"
                 : "=&s"(c), "+s"(done) : "v"(lds), "v"(one) : "scc", "memory");
}
DI void lds_wait_ge(u32 lds, int need) {
    u32 v, c, n;
    asm volatile("s_cmp_lt_i32 %3, 1\n\ts_cbranch_scc1 2f\n\ts_mov_b32 %2, 0x800000\n1:\n\tds_read_b32 %0, %4\n\ts_waitcnt lgkmcnt(0)\n\t"
                 "v_readfirstlane_b32 %1, %0\n\ts_sub_u32 %2, %2, 1\n\ts_cbranch_scc1 2f\n\ts_cmp_lt_u32 %1, %3\n\ts_cbranch_scc1 1b\n2:"
                 : "=&v"(v), "=&s"(c), "=&s"(n) : "s"(need), "v"(lds) : "scc", "memory");
}
template <int S, int N> DI void soft_idle(int t) { asm volatile("s_cmp_gt_u32 %0, %2\n\ts_cbranch_scc1 1f\n\ts_sleep %1\n1:" :: "s"(t), "i"(S), "i"(N) : "scc"); }
#define SCHED() __builtin_amdgcn_sched_barrier(0)
DI bf16x8 lds128(u32 a) { return *(const LAS bf16x8*)(size_t)a; }
DI s16x4 ldstr(u32 a) { return __builtin_amdgcn_ds_read_tr16_b64_v4i16((LAS s16x4*)(size_t)a); }
DI bf16x8 tr_cat(s16x4 lo, s16x4 hi) {
    typedef __attribute__((ext_vector_type(8))) short s16x8;
    s16x8 v = __builtin_shufflevector(lo, hi, 0, 1, 2, 3, 4, 5, 6, 7);
    return __builtin_bit_cast(bf16x8, v);
}
DI f32x4 mfma(bf16x8 a, bf16x8 b, f32x4 c) { return __builtin_amdgcn_mfma_f32_16x16x32_bf16(a, b, c, 0, 0, 0); }
DI void mfma_acc(f32x4& c, bf16x8 a, bf16x8 b) { asm volatile("v_mfma_f32_16x16x32_bf16 %0, %1, %2, %0" : "+v"(c) : "v"(a), "v"(b)); }
DI void mfma_drain() { SCHED(); asm volatile("s_nop 15" ::: "memory"); SCHED(); }
DI u32 pk2(float lo, float hi) { f32x2 v = {lo, hi}; return __builtin_bit_cast(u32, __builtin_convertvector(v, bf16x2)); }
DI u32x2 pk4(f32x4 v) { u32x2 r = {pk2(v[0], v[1]), pk2(v[2], v[3])}; return r; }
DI bf16x8 cat(u32x2 a, u32x2 b) { u32x4 v = {a[0], a[1], b[0], b[1]}; return __builtin_bit_cast(bf16x8, v); }
DI u32x4 widen(u32x2 p0, u32x2 p1) {
    auto x = __builtin_amdgcn_permlane16_swap(p0[0], p1[0], false, false);
    auto y = __builtin_amdgcn_permlane16_swap(p0[1], p1[1], false, false);
    u32x4 r = {x[0], y[0], x[1], y[1]};
    return r;
}
DI float dot2(u32 a, u32 b, float c) { return __builtin_amdgcn_fdot2_f32_bf16(__builtin_bit_cast(bf16x2, a), __builtin_bit_cast(bf16x2, b), c, false); }
template <int CTRL> DI float dppf(float v) { return __uint_as_float(__builtin_amdgcn_update_dpp(0u, __float_as_uint(v), CTRL, 0xf, 0xf, false)); }
DI float bf2f(u32 v) { return __uint_as_float(v << 16); }
DI float rbf(float f) { return bf2f(pk2(f, 0.f) & 0xffffu); }
DI float fmax2(float a, float b) { return __builtin_amdgcn_fmed3f(a, b, __builtin_inff()); }
DI float red4max(float x) {
    auto a = __builtin_amdgcn_permlane16_swap(__float_as_uint(x), __float_as_uint(x), false, false);
    x = fmax2(__uint_as_float(a[0]), __uint_as_float(a[1]));
    auto b = __builtin_amdgcn_permlane32_swap(__float_as_uint(x), __float_as_uint(x), false, false);
    return fmax2(__uint_as_float(b[0]), __uint_as_float(b[1]));
}
DI float red4sum(float x) {
    auto a = __builtin_amdgcn_permlane16_swap(__float_as_uint(x), __float_as_uint(x), false, false);
    x = __uint_as_float(a[0]) + __uint_as_float(a[1]);
    auto b = __builtin_amdgcn_permlane32_swap(__float_as_uint(x), __float_as_uint(x), false, false);
    return __uint_as_float(b[0]) + __uint_as_float(b[1]);
}
DI u32 lane_id() { u32 l = __builtin_amdgcn_mbcnt_hi(~0u, __builtin_amdgcn_mbcnt_lo(~0u, 0u)); asm volatile("" : "+v"(l)); return l; }
DI u32x4 window_attn(bf16x8 Qf, bf16x8 Kp, bf16x8 Kc, bf16x8 V0, bf16x8 V1, bool first, u32 r, u32 g) {
    const float scale = 0.17677669529663687f, sc = 1.4426950408889634f, edge = -1e9f / scale;
    const bool par = r & 1u, cur0 = r >= 4u * g + (r & 1u), cur1 = r >= 4u * g + 2u + (r & 1u);
    const u32 sh = (r & 1u) * 16u;
    const f32x4 z = {0.f, 0.f, 0.f, 0.f};
    const f32x4 sp = mfma(Kp, Qf, z), sn = mfma(Kc, Qf, z);
    float p0 = par ? sp[1] : sp[0], p1 = par ? sp[3] : sp[2];
    p0 = first ? edge : p0; p1 = first ? edge : p1;
    u32 lg = pk2(cur0 ? (par ? sn[1] : sn[0]) : p0, cur1 ? (par ? sn[3] : sn[2]) : p1);
    lg = pk2(bf2f(lg) * scale, __uint_as_float(lg & 0xffff0000u) * scale);
    const float v0 = bf2f(lg), v1 = __uint_as_float(lg & 0xffff0000u);
    const float mx = red4max(fmax2(v0, v1));
    const float e0 = __builtin_amdgcn_exp2f((v0 - mx) * sc), e1 = __builtin_amdgcn_exp2f((v1 - mx) * sc);
    const float inv = __builtin_amdgcn_rcpf(red4sum(e0 + e1));
    const u32 pk = pk2(e0 * inv, e1 * inv), w0 = (pk & 0xffffu) << sh, w1 = (pk >> 16) << sh;
    const u32x4 pf = {cur0 ? 0u : w0, cur1 ? 0u : w1, cur0 ? w0 : 0u, cur1 ? w1 : 0u};
    const bf16x8 Pf = __builtin_bit_cast(bf16x8, pf);
    return widen(pk4(mfma(V0, Pf, z)), pk4(mfma(V1, Pf, z)));
}
DI void poll_ptr(const char* p, u64 tag) {
    const GAS u64* q = (const GAS u64*)p;
#pragma unroll 1
    for (u32 spin = 0; spin < SPIN_MAX; ++spin) {
        const u64 v = __hip_atomic_load(q, __ATOMIC_RELAXED, __HIP_MEMORY_SCOPE_AGENT);
        if (__all(v == tag)) break;
        __builtin_amdgcn_s_sleep(2);
    }
    asm volatile("" ::: "memory");
}

constexpr u32 K1_W = 0, K1_WSZ = 2 * 8192;
constexpr u32 K1_A = 2 * K1_WSZ, K1_ASZ = 128 * 128;
constexpr u32 K1_Q = K1_A + 3 * K1_ASZ, K1_QSZ = 3 * 8192;
constexpr u32 CTL = K1_Q + 3 * K1_QSZ;
constexpr u32 SCR = CTL + 64;
constexpr u32 HS0 = SCR + 512, HS1 = K1_Q + 16384;
constexpr u32 LDS_BYTES = HS0 + 4096;
DI void touch(const u16* p) { __builtin_amdgcn_global_load_lds((const u32*)p, (LAS u32*)(size_t)SCR, 4, 0, 0); }
DI void lds_wait4(u32 a, u32 need) {
    u32 v0, v1, v2, v3, c, n;
    asm volatile("s_mov_b32 %5, 0x800000\n1:\n\tds_read_b32 %0, %6\n\tds_read_b32 %1, %6 offset:4\n\tds_read_b32 %2, %6 offset:8\n\tds_read_b32 %3, %6 offset:12\n\t"
                 "s_waitcnt lgkmcnt(0)\n\tv_min_u32 %0, %0, %1\n\tv_min3_u32 %0, %0, %2, %3\n\ts_nop 0\n\tv_readfirstlane_b32 %4, %0\n\ts_sub_u32 %5, %5, 1\n\t"
                 "s_cbranch_scc1 2f\n\ts_cmp_ge_u32 %4, %7\n\ts_cbranch_scc1 2f\n\ts_sleep 1\n\ts_branch 1b\n2:"
                 : "=&v"(v0), "=&v"(v1), "=&v"(v2), "=&v"(v3), "=&s"(c), "=&s"(n) : "v"(a), "s"(need) : "scc", "memory");
}
DI const u16* code_line(u32 i) {
    u64 pc; int fwd, len;
    asm volatile("s_getpc_b64 %0\n1:\n\ts_mov_b32 %1, .Lpc_end-1b\n\ts_mov_b32 %2, .Lpc_end-.Lpc_begin" : "=s"(pc), "=s"(fwd), "=s"(len));
    return (const u16*)(pc + (long long)fwd - ((int)(i * 128u) < len ? i * 128u : 0u));
}
constexpr u32 K2A[4] = {K1_W + K1_WSZ, K1_A + 2 * K1_ASZ, K1_A, K1_A + K1_ASZ}, K2B[4] = {K1_W, K1_Q + 2 * K1_QSZ, K1_Q, K1_Q + K1_QSZ};
constexpr int K2_ND = 32, K2_NT = 40;
constexpr int pend(int s) { return s < 0 ? 0 : s < 4 ? 4 * (s + 1) : s > 39 ? 304 : 16 + 8 * (s - 3); }
struct Cum { int v[41]; };
constexpr Cum make_cum() {
    Cum c{};
    c.v[0] = 8;
    for (int i = -1; i < 39; ++i) {
        const int prev = c.v[i + 1], hi = pend(i + 4), lo = pend(i + 3) - (i >= 24 ? 4 : 0);
        int n = (i <= 0 || i == 15) ? hi - prev : 7 + (i & 1);
        if (prev + n < lo) n = lo - prev;
        if (prev + n > hi) n = hi - prev;
        c.v[i + 2] = prev + n;
    }
    return c;
}
constexpr Cum CUM = make_cum();
constexpr int first_int(int p) { int i = -2; while (CUM.v[i + 2] <= p) ++i; return i; }
template <int I, int N, class F> DI void static_for(F f) { if constexpr (I < N) { f(std::integral_constant<int, I>{}); static_for<I + 1, N>(f); } }

#define GROUP(n, m1, m2, m3, m4, F1, F2, F3, F4, F5, F6, F7, F8)                                                            \
    MM(n, m1, 0); F1; MM(n, m1, 1); F2; MM(n, m2, 1); F3; MM(n, m2, 0); F4; MM(n, m3, 0); F5; MM(n, m3, 1); F6; MM(n, m4, 1); F7; MM(n, m4, 0); F8;
#define UPD(x) do { x; SCHED(); } while (0)
#define NIL (void)0

__global__ __launch_bounds__(512, 2) void fused(const u16* x, const u16* wqkv, const u16* wo,
                                                const u16* cr, const u16* ci, const u16* wre,
                                                const u16* wim, const u16* gw, char* ws, u16* out) {
    const int b = blockIdx.x, xcd = b & 7, rr = b >> 3;
    const int ti = rr >> 2, tj = rr & 3, xm = xcd >> 2, xn = xcd & 3;
    const int tm = xm * 8 + ti, tn = xn * 4 + tj, s0 = tm * 128;
    const int hf = xcd >> 2, ui = rr >> 3, un = rr & 7, um = (xcd & 3) * 4 + ui;
    const int wid = __builtin_amdgcn_readfirstlane(threadIdx.x >> 6);
    asm volatile("" : "+s"(x), "+s"(wqkv), "+s"(wo), "+s"(cr), "+s"(ci), "+s"(wre), "+s"(wim), "+s"(gw), "+s"(ws), "+s"(out));
    asm volatile(".Lpc_begin:");
    if (__builtin_expect(wid >= 4, 1)) {
        const int lw = wid - 4;
        const u32 lane = threadIdx.x & 63;
        const u32 l3 = lane >> 3, l7 = lane & 7, l4 = lane >> 4, sw16 = (l7 ^ l4) << 4;
        const rsrc_t rx = mk_rsrc(x, 2048u * 2048u), rq = mk_rsrc(wqkv, 1024u * 6144u);
        const u32 vx0 = l3 * 2048u + sw16, vx1 = (vx0 ^ 64u) + 15360u, vx2 = vx0 + 30720u, vx3 = vx1 + 30720u;
        const u32 vq0 = l3 * 6144u + ((l7 ^ ((l4 & 1u) << 1)) << 4), vq1 = (vq0 ^ 64u) + 8u * 6144u;
        const u32 sa = (u32)(s0 + 32 * lw) * 2048u, sq = (u32)(16 * lw) * 6144u + (u32)tn * 128u;
        const int cl1 = (ti + tj) & 3;
        u32 k1d[4];
#pragma unroll
        for (int i = 0; i < 4; ++i) k1d[i] = (u32)(i + (i == cl1 ? 4 : 0)) * 128u;
        auto kb = [&](int i) { u32 v = k1d[i & 3] + (u32)(i & 12) * 128u; asm("" : "+s"(v)); return v & 0x780u; };
        auto ldXa = [&](u32 l, u32 o) { dma16<0>(rx, l, vx0, sa + o); dma16<1024>(rx, l, vx1, sa + o); };
        auto ldXb = [&](u32 l, u32 o) { dma16<2048>(rx, l, vx2, sa + o); dma16<3072>(rx, l, vx3, sa + o); };
        const u32 gam = (ti >> 2) & 1, vqa = gam ? vq1 : vq0, vqb = gam ? vq0 : vq1, lqa = gam * 1024u, lqb = 1024u - lqa;
        auto ldQa = [&](u32 l, u32 o) { dma16<0>(rq, l + lqa, vqa, o); };
        auto ldQb = [&](u32 l, u32 o) { dma16<0>(rq, l + lqb, vqb, o); };
        auto ldXQa = [&](u32 lx, u32 ox, u32 lq, u32 oq) { ldQa(lq, oq); ldXa(lx, ox); ldQa(lq + 8192u, oq + 2048u); ldXb(lx, ox); };
        auto ldXQb = [&](u32 lq, u32 oq) { ldQa(lq + 16384u, oq + 4096u); ldQb(lq, oq); ldQb(lq + 8192u, oq + 2048u); ldQb(lq + 16384u, oq + 4096u); };
        auto ldXQ = [&](u32 lx, u32 ox, u32 lq, u32 oq) { ldXQa(lx, ox, lq, oq); ldXQb(lq, oq); };
        const u32 pX0 = K1_A + lw * 4096u, pQ0 = K1_Q + lw * 2048u;
        auto pwp = [&](u32 sl) { return (hf ? ci : cr) + (size_t)((xcd & 3) * 512 + (sl & 31u) * 16u + (lane >> 2)) * 2048 + (sl >> 5) * 256u + (lane & 3u) * 64u; };
        const int pwa = (1 - hf) * 1024, pwb = 1024 - pwa;
        {
            const rsrc_t rre = mk_rsrc(wre, 1024u * 2048u), rim = mk_rsrc(wim, 1024u * 2048u);
            const u32 sw = (u32)(64 * tn + 16 * lw) * 2048u, pW0 = K1_W + lw * 2048u;
            auto ldW = [&](u32 l, u32 o) { dma16<0>(rre, l, vx0, o); dma16<1024>(rre, l, vx1, o); dma16<0>(rim, l + 8192u, vx0, o); dma16<1024>(rim, l + 8192u, vx1, o); };
            ldXQ(pX0, kb(0), pQ0, sq + kb(0) * 3072u); ldW(pW0, sw + kb(0));
            ldXQa(pX0 + K1_ASZ, kb(1), pQ0 + K1_QSZ, sq + kb(1) * 3072u);
            wait_vm<6>(); bar();
            ldXQb(pQ0 + K1_QSZ, sq + kb(1) * 3072u);
            ldW(pW0 + K1_WSZ, sw + kb(1));
            ldXQa(pX0 + 2 * K1_ASZ, kb(2), pQ0 + 2 * K1_QSZ, sq + kb(2) * 3072u);
            const u32 slot = (u32)((xn & 1) ? rr : ti * 3 + tj) * 4u + lw;
            const u16* pw0 = pwp(slot & 127u);
            const u16* pw1 = pwp((96u + ti * 4u + lw) & 127u);
            int tp = (tn & 7) != 3 ? (cl1 + 3) & 3 : -1, tp2 = (xn & 1) | tj;
            const char* ptw = (const char*)((gam ? wim : wre) + (size_t)(64 * tn + lane) * 1024);
            int twc = lw == 2 ? cl1 : -1;
            asm volatile("" : "+v"(ptw), "+s"(twc));
            u32 oW = sw + kb(2), oX = kb(3), oQ = sq + kb(3) * 3072u;
            asm volatile("" : "+s"(tp), "+s"(tp2), "+v"(pw0), "+v"(pw1), "+s"(oW), "+s"(oX), "+s"(oQ));
#pragma unroll
            for (int t = 0; t < 12; ++t) {
                if (t == 0) wait_vm<6>(); else wait_vm<10>();
                bar();
                SCHED();
                if (t == 0) ldXQb(pQ0 + 2 * K1_QSZ, sq + kb(2) * 3072u);
                ldW(pW0 + (t & 1) * K1_WSZ, oW);
                if (t < 9 && ((t + 3) & 3) == twc) touch((const u16*)(ptw + oX));
                ldXQ(pX0 + t % 3 * K1_ASZ, oX, pQ0 + t % 3 * K1_QSZ, oQ);
                SCHED();
                if (t >= 2 && t < 10 && (t & 3) == tp) { touch(pw0 + (t >= 6 ? pwb : pwa)); if (tp2 == 0) touch(pw1 + (t >= 6 ? pwb : pwa)); }
                if (t == 10 && (rr & 15) == 1) touch(code_line(lw * 64u + lane));
                const int sn = t + 4;
                oW = sw + oX; oX = kb(sn < 12 ? sn : sn == 12 ? 15 : sn - 1); oQ = sq + kb(sn < 12 ? sn : sn == 12 ? 14 : sn - 1) * 3072u;
                asm volatile("" : "+s"(oW), "+s"(oX), "+s"(oQ));
            }
            wait_vm<10>(); bar();
            ldW(pW0, oW); ldXa(pX0, oX); ldXb(pX0, oX);
            wait_vm<4>(); bar();
            ldW(pW0 + K1_WSZ, sw + oX);
            wait_vm<0>(); bar();
            bar();
        }
        const rsrc_t rws = mk_rsrc(ws, WS_FLAG), rc = mk_rsrc(hf ? ci : cr, 2048u * 4096u);
        const u64 tag = *(const LAS u64*)(size_t)(CTL + 40);
        const int cls = (ui + un) & 3;
        const u32 va0 = l3 * 4096u + sw16, va1 = (va0 ^ 64u) + 31744u, va2 = va0 + 63488u, va3 = va1 + 63488u;
        const u32 vb0 = lane * 16u, vb1 = vb0 + EBS - 2048u, vo1 = vb0 + OTS - 2048u, lo = lw * 4096u;
        const int lr = lw ^ (1 - hf);
        const u32 sA0 = (u32)(128 * um + 32 * lr) * 4096u, sB0 = WS_BH + hf * BH_HALF + (u32)(8 * un + 2 * lw) * EBS, sO0 = WS_O + (u32)(8 * um + 2 * lr) * OTS + hf * 16384u;
        auto ldC = [&](u32 l, u32 o) { dma16<0>(rc, l + lo, va0, o); dma16<1024>(rc, l + lo, va1, o); dma16<2048>(rc, l + lo, va2, o); dma16<3072>(rc, l + lo, va3, o); };
        auto ldF = [&](u32 l, u32 v1, u32 o) { dma16<0>(rws, l + lo, vb0, o); dma16<1024>(rws, l + lo, vb0, o); dma16<2048>(rws, l + lo, v1, o); dma16<3072>(rws, l + lo, v1, o); };
        u32 kd[4];
#pragma unroll
        for (int i = 0; i < 4; ++i) kd[i] = (u32)(i + (i == cls ? 4 : 0)) * 128u;
        auto ka = [&](int t) { u32 v = kd[t & 3] + (u32)(t >> 2) * 512u + (u32)(1 - hf) * 2048u; asm("" : "+s"(v)); return v & 0xf80u; };
        ldC(K2A[0], sA0 + ka(0));
        const u32 nfw = lw >= 2 ? 1u : (b | lw) ? 16u : 32u;
        const char* pfw = ws + (lw == 0 ? F_O + (um * 16 + (lane & (nfw - 1u))) * 128u : lw == 1 ? F_AB + xcd * 32768 + un * 4096 + ((lane & 8u) * 2u + hf * 8 + (lane & 7u)) * 128u
                                                                             : F_H + (u32)((tm > 0 ? tm - 1 : 0) * 16 + tn) * 128u);
        const u32 lfw = SCR + (lw & 2) * 128u + (lw == 3 ? 16u : 0u);
        auto look = [&]() { if (lane < nfw) __builtin_amdgcn_global_load_lds((const u32*)pfw, (LAS u32*)(size_t)lfw, 16, 0, SC1); };
        u32 lck = lfw + (lane & (nfw - 1u)) * 16u;
        u64 seen = 0;
        auto peek = [&]() { asm volatile("ds_read_b64 %0, %1" : "=v"(seen) : "v"(lck) : "memory"); };
        auto check = [&]() { if (!__all(seen == tag)) poll_ptr(pfw, tag); };
        const bool halo = lw >= 2 && tm > 0;
        const rsrc_t rwo = mk_rsrc(wo, 1024u * 2048u);
        const u32 vw0 = l4 * 2048u + (((lane & 15u) ^ (l4 << 1)) << 4);
        u32 vW[4] = {vw0, vw0 + 7168u, (vw0 ^ 128u) + 14336u, (vw0 ^ 128u) + 21504u}, vA[4] = {va0, va1, va2, va3};
        const u32 sW0 = (u32)(512 * hf + 16 * lw) * 2048u + (u32)un * 256u;
        u32 oA[K2_NT], oB[K2_NT];
        auto prep = [&](int s) {
            if (s < K2_ND) { const u32 k = ka(s); oA[s] = sA0 + k; oB[s] = sB0 + k * 16u; }
            else { u32 k = kd[s & 3] + (u32)(s & 4) * 128u; asm("" : "+s"(k)); k &= 0x380u; oA[s] = sO0 + k * 16u; oB[s] = sW0 + k * 1024u; }
            asm volatile("" : "+s"(oA[s]), "+s"(oB[s]));
        };
        constexpr int CK1 = first_int(pend(11)) - 1, CKH = 12, CKO = first_int(pend(31) + 4) - 1;
        static_assert(pend(CK1 + 1) > CUM.v[CK1 - 4 + 2] && pend(CKH + 1) > CUM.v[CKH - 4 + 2] && pend(CKO + 1) > CUM.v[CKO - 4 + 2] && pend(16) > CUM.v[CKH + 1] + 4, "");
        static_for<0, K2_NT>([&](auto S) { if constexpr (first_int(pend(S - 1)) <= -1) prep(S); });
        asm volatile("" : "+v"(pfw), "+v"(lck), "+v"(vW[0]), "+v"(vW[1]), "+v"(vW[2]), "+v"(vW[3]));
        if (lw == 1) {
            lds_wait4(CTL + 16, 1u);
            if (lane < 8) __hip_atomic_store((GAS u64*)(ws + F_AB + lane * 32768u + (tn * 16 + tm) * 128), tag, __ATOMIC_RELAXED, __HIP_MEMORY_SCOPE_AGENT);
            poll_ptr(ws + F_AB + xcd * 32768 + un * 4096 + ((lane & 8u) * 2u + (1 - hf) * 8 + (lane & 7u)) * 128u, tag);
            *(volatile LAS u32*)(size_t)(CTL + 32) = 1u;
        } else {
            lds_wait4(CTL, 1u);
            const u32 lx = pX0 + K1_ASZ, lq = pQ0 + K1_QSZ, ks = kb(15);
            ldXQ(lx, ks, lq, sq + ks * 3072u);
            if (lw == 0) ldXQ(lx + 4096u, ks + 65536u, lq + 2048u, sq + ks * 3072u + 98304u);
            wait_vm<0>();
            if (lane == 0) __hip_atomic_fetch_add((LAS u32*)(size_t)(CTL + 48), lw == 0 ? 2u : 1u, __ATOMIC_RELAXED, __HIP_MEMORY_SCOPE_WORKGROUP);
        }
        float gate = 0.f;
        if (lw == 0) {
            const u32 d1 = (u32)((1 ^ (1 - hf)) - lr) * 131072u;
            lds_wait4(CTL, 2u); ldC(K2A[1], sA0 + ka(1)); ldC(K2A[1] + 4096u, sA0 + d1 + ka(1));
            lds_wait4(CTL, 3u); ldC(K2A[2], sA0 + ka(2)); ldC(K2A[2] + 4096u, sA0 + d1 + ka(2));
            lds_wait4(CTL, 4u); ldC(K2A[3], sA0 + ka(3)); ldC(K2A[3] + 4096u, sA0 + d1 + ka(3));
        } else if (lw >= 2) {
            const u16* pgw = gw + lane * 8;
            const u16* pgx = x + (size_t)(s0 + 8 * tn + 4 * (lw - 2) + 2) * 1024 + lane * 8;
            u32x4 gv[10];
            lds_wait4(CTL, 2u); ldC(K2A[1], sA0 + ka(1));
            asm volatile("global_load_dwordx4 %0, %10, off\n\tglobal_load_dwordx4 %1, %10, off offset:1024\n\t"
                         "global_load_dwordx4 %2, %11, off offset:-4096\n\tglobal_load_dwordx4 %3, %11, off offset:-3072\n\t"
                         "global_load_dwordx4 %4, %11, off offset:-2048\n\tglobal_load_dwordx4 %5, %11, off offset:-1024\n\t"
                         "global_load_dwordx4 %6, %11, off\n\tglobal_load_dwordx4 %7, %11, off offset:1024\n\t"
                         "global_load_dwordx4 %8, %11, off offset:2048\n\tglobal_load_dwordx4 %9, %11, off offset:3072"
                         : "=&v"(gv[0]), "=&v"(gv[1]), "=&v"(gv[2]), "=&v"(gv[3]), "=&v"(gv[4]), "=&v"(gv[5]), "=&v"(gv[6]), "=&v"(gv[7]), "=&v"(gv[8]), "=&v"(gv[9])
                         : "v"(pgw), "v"(pgx) : "memory");
            lds_wait4(CTL, 3u); ldC(K2A[2], sA0 + ka(2));
            lds_wait4(CTL, 4u); ldC(K2A[3], sA0 + ka(3));
            __builtin_amdgcn_s_setprio(3);
            asm volatile("s_waitcnt vmcnt(8)" : "+v"(gv[0]), "+v"(gv[1]), "+v"(gv[2]), "+v"(gv[3]), "+v"(gv[4]), "+v"(gv[5]), "+v"(gv[6]), "+v"(gv[7]), "+v"(gv[8]), "+v"(gv[9]) :: "memory");
            float a[4];
#pragma unroll
            for (int q = 0; q < 4; ++q) {
                float t = 0.f;
#pragma unroll
                for (int j = 0; j < 8; ++j) {
                    const u32 xv = gv[2 + 2 * q + (j >> 2)][j & 3], wv = gv[j >> 2][j & 3];
                    t = dot2(pk2(bf2f(xv) * bf2f(wv), __uint_as_float(xv & 0xffff0000u) * __uint_as_float(wv & 0xffff0000u)), 0x3f803f80u, t);
                }
                a[q] = red4sum(t);
            }
            const float b0 = ((lane & 1) ? a[1] : a[0]) + dppf<0xb1>((lane & 1) ? a[0] : a[1]), b1 = ((lane & 1) ? a[3] : a[2]) + dppf<0xb1>((lane & 1) ? a[2] : a[3]);
            gate = ((lane & 2) ? b1 : b0) + dppf<0x4e>((lane & 2) ? b0 : b1);
            gate += dppf<0x124>(gate); gate += dppf<0x128>(gate);
            gate = rbf(__builtin_amdgcn_rcpf(1.f + __expf(-rbf(gate))));
            asm volatile("" : "+v"(gate));
            __builtin_amdgcn_s_setprio(0);
        }
        if (lw != 1) lds_wait_ge(CTL + 32, 1); else lds_wait4(CTL, 2u);
        ldF(K2B[0], vb1, oB[0]); ldF(K2B[1], vb1, oB[1]);
        static_for<-1, 39>([&](auto I) {
            constexpr int i = I, p0 = CUM.v[i + 1], p1 = CUM.v[i + 2], mid = p1 - p0 > 4 ? p0 + 4 : p1;
            wait_vm<p0 - pend(i + 1)>();
            bar();
            SCHED();
            if constexpr (i == CK1 || i == CKH || i == CKO) peek();
            auto piece = [&](auto P) {
                constexpr int p = P, st = p < 16 ? p / 4 : 4 + (p - 16) / 8, q = p < 16 ? p & 3 : (p - 16) & 7, j = q & 3;
                if constexpr (q < 4) {
                    if constexpr (st < K2_ND) dma16<j * 1024>(rws, K2B[st & 3] + lo, j < 2 ? vb0 : vb1, oB[st]);
                    else dma16<j * 1024>(rwo, K2B[st & 3] + lo, vW[j], oB[st]);
                } else {
                    if constexpr (st < K2_ND) dma16<j * 1024>(rc, K2A[st & 3] + lo, vA[j], oA[st]);
                    else dma16<j * 1024>(rws, K2A[st & 3] + lo, j < 2 ? vb0 : vo1, oA[st]);
                }
            };
            static_for<p0, mid>(piece);
            SCHED();
            static_for<2, K2_NT>([&](auto S) { if constexpr (first_int(pend(S - 1)) == i + 1) prep(S); });
            if constexpr (i == CK1 || i == CKH || i == CKO) asm volatile("s_waitcnt lgkmcnt(0)" : "+v"(seen) :: "memory");
            if constexpr (i == -1) { if (lw >= 2) __builtin_amdgcn_raw_buffer_store_b32(__float_as_uint(gate), rws, lane < 4 ? lane * 4u : 0x80000000u, WS_G + (u32)(s0 + 8 * tn + 4 * (lw - 2)) * 4u, SC1); }
            if constexpr (i == 15) { if ((rr & 15) == 1) touch(code_line(lw * 64u + lane)); }
            if constexpr (i == CK1 - 4) { if (lw == 1) look(); }
            if constexpr (i == CK1) { if (lw == 1) check(); }
            if constexpr (i == CKH - 4) { if (halo) look(); }
            if constexpr (i == CKH) {
                if (halo) {
                    check();
                    const u32 o = WS_H + (u32)((tm - 1) * 32 + 2 * tn + lw - 2) * 2048u;
                    dma16<0>(rws, HS1 + (lw - 2) * 2048u, vb0, o); dma16<1024>(rws, HS1 + (lw - 2) * 2048u, vb0, o);
                }
            }
            if constexpr (i == CKO - 4) { if (lw == 0) look(); }
            if constexpr (i == CKO) { if (lw == 0) { check(); if (lane < 32) dma16<0>(rws, HS0, vb0, WS_G + (u32)um * 512u); } }
            SCHED();
            static_for<mid, p1>(piece);
            if constexpr (i == 34) { if (lw == 3 && (rr & 7) == 1 && lane < 32) touch(code_line(lane)); }
        });
        if (b == 0 && lw == 0 && lane == 0) __hip_atomic_store((GAS u64*)(ws + F_EPOCH), tag, __ATOMIC_RELAXED, __HIP_MEMORY_SCOPE_AGENT);
        return;
    }
    const u64 ep = __hip_atomic_load((const GAS u64*)(ws + F_EPOCH), __ATOMIC_RELAXED, __HIP_MEMORY_SCOPE_AGENT);
    const rsrc_t rws = mk_rsrc(ws, WS_END);
    if ((rr & 15) == 1) touch(code_line(threadIdx.x));
    u64 tag;
    const int hh = wid & 1, mh = wid >> 1;
    bf16x8 Q1, K1f; u32x2 V1[2];
    {
        f32x4 acc[10][5];
        {
            const u32 lane = threadIdx.x & 63;
            const u32 r_ = lane & 15, g_ = lane >> 4, q4 = r_ >> 2, p4 = lane & 3;
            const u32 rs0 = r_ * 128u + ((g_ ^ (r_ >> 1)) << 4), rs1 = rs0 ^ 64u;
            const u32 swb = (((q4 >> 1) & 1u) << 1) | ((g_ & 1u) << 2);
            const u32 tq0 = (8u * g_ + q4) * 128u + (((4u * hh + (p4 >> 1)) ^ swb) << 4) + (p4 & 1u) * 8u, tq1 = tq0 ^ 32u;
            const u32 offA = K1_A + mh * 8192u, offW = K1_W + hh * 4096u;
#pragma unroll
            for (int n = 0; n < 10; ++n)
#pragma unroll
                for (int m = 1; m < 5; ++m) { acc[n][m] = (f32x4){0.f, 0.f, 0.f, 0.f}; asm volatile("" : "+v"(acc[n][m])); }
            bf16x8 A[2][5], B[2][10];
            s16x4 T[2][6][2];
#define FQ(n, h) tr_cat(T[h][(n) < 6 ? (n) : 0][0], T[h][(n) < 6 ? (n) : 0][1])
#define MM(n, m, h) do { if ((n) < 4) mfma_acc(acc[n][m], FQ(n, h), A[h][m]); else if ((n) < 6) mfma_acc(acc[n][m], A[h][m], FQ(n, h)); else mfma_acc(acc[n][m], A[h][m], B[h][n]); SCHED(); } while (0)
#define RDA(m, h) do { A[h][m] = lds128(((h) ? a1 : a0) + ((m) - 1) * 2048u); SCHED(); } while (0)
#define RDQ(n, h, t) do { T[h][n][t] = ldstr((((n) & 1) ? qb1 : qb0) + ((n) >> 1) * 8192u + (h) * 4096u + (t) * 512u); SCHED(); } while (0)
#define RDW(n, h) do { B[h][n] = lds128(((h) ? w1 : w0) + (((n) - 6) >> 1) * 8192u + ((n) & 1) * 2048u); SCHED(); } while (0)
#define RDQ4(n) RDQ(n, 0, 0), RDQ(n, 0, 1), RDQ(n, 1, 0), RDQ(n, 1, 1)
#define GROUPX(...) GROUP(__VA_ARGS__)
#define NEXTA RDA(1, 0), RDA(1, 1), RDA(2, 1), RDA(2, 0), RDA(3, 0), RDA(3, 1), RDA(4, 1), RDA(4, 0)
            u32 a0 = offA + rs0, a1 = offA + rs1, qb0 = K1_Q + tq0, qb1 = K1_Q + tq1, w0 = offW + rs0, w1 = offW + rs1;
            asm volatile("" : "+v"(a0), "+v"(a1), "+v"(qb0), "+v"(qb1), "+v"(w0), "+v"(w1));
            SCHED();
            tag = (((u64)__builtin_amdgcn_readfirstlane((u32)(ep >> 32)) << 32) | (u32)__builtin_amdgcn_readfirstlane((u32)ep)) * 6364136223846793005ull + 1442695040888963407ull;
            if (threadIdx.x < 15 && (threadIdx.x >> 1) != 5) ((LAS u32*)(size_t)CTL)[threadIdx.x] = 0u;
            if (threadIdx.x == 10) *(LAS u64*)(size_t)(CTL + 40) = tag;
            asm volatile("" : "+s"(tag));
            wait_lgkm0();
            bar();
            RDW(6, 0); RDW(6, 1); RDA(1, 0); RDA(1, 1); RDA(2, 1); RDA(2, 0); RDA(3, 0); RDA(3, 1); RDA(4, 1); RDA(4, 0);
            int s3 = 0;
#define TAIL_AB                                                                                                             \
    GROUP(7, 1, 2, 3, 4, RDW(8, 0), RDW(8, 1), RDW(9, 0), RDW(9, 1), UPD(w0 ^= K1_WSZ), UPD(w1 ^= K1_WSZ), NIL, NIL)        \
    wait_lgkm0(); SCHED(); bar(); SCHED();                                                                                  \
    GROUP(8, 4, 3, 2, 1, RDW(6, 0), RDW(6, 1), NIL, NIL, NIL, NIL, NIL, NIL)                                                \
    GROUPX(9, 1, 2, 3, 4, NEXTA)
#pragma unroll 1
            for (int t = 0; t < 12; ++t) {
                u32 da, dq;
                GROUPX(6, 1, 2, 3, 4, RDQ4(0), UPD(da = s3 == 2 ? (u32)(-2 * (int)K1_ASZ) : K1_ASZ), UPD(a0 += da), UPD(a1 += da), NIL)
                GROUPX(0, 4, 3, 2, 1, RDQ4(1), UPD(dq = s3 == 2 ? (u32)(-2 * (int)K1_QSZ) : K1_QSZ), UPD(s3 = s3 == 2 ? 0 : s3 + 1), NIL, NIL)
                GROUPX(1, 1, 2, 3, 4, RDQ4(2), NIL, NIL, NIL, NIL)
                GROUPX(2, 4, 3, 2, 1, RDQ4(3), NIL, NIL, NIL, NIL)
                GROUPX(3, 1, 2, 3, 4, RDQ4(4), NIL, NIL, NIL, NIL)
                GROUPX(4, 1, 2, 3, 4, RDQ4(5), UPD(qb0 += dq), UPD(qb1 += dq), NIL, NIL)
                GROUP(5, 4, 3, 2, 1, RDW(7, 0), RDW(7, 1), NIL, NIL, NIL, NIL, NIL, NIL)
                TAIL_AB
                soft_idle<14, 1>(t);
            }
#pragma unroll 1
            for (int u = 0; u < 3; ++u) {
                u32 da;
                GROUP(6, 1, 2, 3, 4, RDW(7, 0), RDW(7, 1), UPD(da = s3 == 2 ? (u32)(-2 * (int)K1_ASZ) : K1_ASZ), UPD(a0 += da), UPD(a1 += da), UPD(s3 = s3 == 2 ? 0 : s3 + 1), NIL, NIL)
                TAIL_AB
            }
            {
                const u32 ln = lane_id(), dl = (((((ln >> 4) & 1u) << 1) | (ln >> 5)) * 16u + (ln & 15u)) * 16u;
                const u32 dst = WS_BH + (u32)(4 * tn + 2 * hh) * EBS + (u32)((s0 >> 5) + 2 * mh) * 1024u;
                u32x2 e0, e1; u32x4 ew;
#define E0(c, pz) UPD({ e0 = pk4(acc[6 + (c)][1 + 2 * (pz)]); asm volatile("" : "+v"(e0)); })
#define E1(c, pz) UPD({ e1 = pk4(acc[6 + (c)][2 + 2 * (pz)]); asm volatile("" : "+v"(e1)); })
#define E2 UPD({ ew = widen(e0, e1); asm volatile("" : "+v"(ew)); })
#define E3(c, pz) UPD(__builtin_amdgcn_raw_buffer_store_b128(ew, rws, dl + (pz) * 1024u, dst + ((c) >> 1) * BH_HALF + ((c) & 1) * EBS, SC1))
#define BOTH(x, y) do { x; y; } while (0)
                SCHED();
                GROUP(6, 1, 2, 3, 4, RDW(7, 0), RDW(7, 1), RDW(8, 0), RDW(8, 1), RDW(9, 0), RDW(9, 1), UPD(a0 += K1_ASZ), UPD(a1 += K1_ASZ))
                GROUP(7, 1, 2, 3, 4, UPD(qb0 += K1_QSZ), BOTH(UPD(qb1 += K1_QSZ), E0(0, 0)), E1(0, 0), BOTH(E2, E3(0, 0)), E0(0, 1), E1(0, 1), E2, E3(0, 1))
                wait_lgkm0(); SCHED(); bar(); SCHED();
                GROUP(8, 4, 3, 2, 1, BOTH(RDQ(0, 0, 0), E0(1, 0)), BOTH(RDQ(0, 0, 1), E1(1, 0)), BOTH(RDQ(0, 1, 0), E2), BOTH(RDQ(0, 1, 1), E3(1, 0)), E0(1, 1), E1(1, 1), E2, E3(1, 1))
                GROUP(9, 1, 2, 3, 4, BOTH(RDA(1, 0), E1(2, 1)), BOTH(RDA(1, 1), E0(2, 1)), BOTH(RDA(2, 1), E2), BOTH(RDA(2, 0), E3(2, 1)),
                      BOTH(RDA(3, 0), E1(2, 0)), BOTH(RDA(3, 1), E0(2, 0)), BOTH(RDA(4, 1), E2), BOTH(RDA(4, 0), E3(2, 0)))
                E0(3, 0); E1(3, 0); E2; E3(3, 0); E0(3, 1);
                asm volatile("s_nop 3"); SCHED();
                E1(3, 1); E2; E3(3, 1);
                s3 = 1;
#undef E0
#undef E1
#undef E2
#undef E3
#undef BOTH
            }
            u32 pc = CTL + wid * 4u;
            asm volatile("" : "+v"(pc));
            u32 one = 1u, pl = CTL + 48, done = 0u;
            asm volatile("" : "+v"(one), "+v"(pl));
#pragma unroll 1
            for (int u = 0; u < 4; ++u) {
                u32 da, dq;
                GROUPX(0, 1, 2, 3, 4, RDQ4(1), UPD(da = s3 == 2 ? (u32)(-2 * (int)K1_ASZ) : K1_ASZ), UPD(a0 += da), UPD(a1 += da), UPD(note_if_drained(done, pc, one)))
                GROUPX(1, 4, 3, 2, 1, RDQ4(2), UPD(dq = s3 == 2 ? (u32)(-2 * (int)K1_QSZ) : K1_QSZ), UPD(s3 = s3 == 2 ? 0 : s3 + 1), NIL, NIL)
                GROUPX(2, 1, 2, 3, 4, RDQ4(3), RDQ4(4))
                GROUPX(3, 4, 3, 2, 1, RDQ4(5), UPD(qb0 += dq), UPD(qb1 += dq), NIL, NIL)
                wait_lgkm0(); SCHED(); *(volatile LAS u32*)(size_t)pc = (u32)(u + 1); note_if_drained(done, pc, one); lds_wait_ge(pl, u == 2 ? 4 : 0); SCHED();
                GROUPX(4, 4, 3, 2, 1, RDQ4(0), NIL, NIL, NIL, NIL)
                GROUPX(5, 1, 2, 3, 4, NEXTA)
            }
            wait_vm<0>();
            *(volatile LAS u32*)(size_t)(pc + 16u) = 1u;
            mfma_drain();
#undef FQ
#undef MM
#undef RDA
#undef RDQ
#undef RDW
#undef RDQ4
#undef NEXTA
#undef TAIL_AB
        }
        const u32 lane = lane_id(), r = lane & 15, g = lane >> 4, gp = ((g & 1u) << 1) | (g >> 1);
        bf16x8 Kf[5]; u32x2 Vp[2][5];
#pragma unroll
        for (int m = 4; m >= 1; --m) {
            Kf[m] = cat(pk4(acc[2][m]), pk4(acc[3][m])); Vp[0][m] = pk4(acc[4][m]); Vp[1][m] = pk4(acc[5][m]);
            if (m == 4) {
                const u32x4 kk = __builtin_bit_cast(u32x4, Kf[4]), vv = {Vp[0][4][0], Vp[0][4][1], Vp[1][4][0], Vp[1][4][1]};
                if (mh) {
                    const u32 o = WS_H + (u32)(tm * 32 + 2 * tn + hh) * 2048u;
                    __builtin_amdgcn_raw_buffer_store_b128(kk, rws, lane * 16u, o, SC1); __builtin_amdgcn_raw_buffer_store_b128(vv, rws, lane * 16u + 1024u, o, SC1);
                } else {
                    *(LAS u32x4*)(size_t)(HS0 + hh * 2048u + lane * 16u) = kk; *(LAS u32x4*)(size_t)(HS0 + hh * 2048u + 1024u + lane * 16u) = vv;
                }
            }
        }
#pragma unroll
        for (int m = 2; m < 5; ++m)
            __builtin_amdgcn_raw_buffer_store_b128(
                window_attn(cat(pk4(acc[0][m]), pk4(acc[1][m])), Kf[m - 1], Kf[m], cat(Vp[0][m - 1], Vp[0][m]), cat(Vp[1][m - 1], Vp[1][m]), false, r, g),
                rws, (gp * 16u + r) * 16u, WS_O + (u32)(8 * tm + 4 * mh + m - 1) * OTS + (u32)(2 * tn + hh) * 1024u, SC1);
        Q1 = cat(pk4(acc[0][1]), pk4(acc[1][1])); K1f = Kf[1]; V1[0] = Vp[0][1]; V1[1] = Vp[1][1];
        asm volatile("" : "+v"(Q1), "+v"(K1f), "+v"(V1[0]), "+v"(V1[1]));
    }
    const int wm = wid >> 1, wn = wid & 1;
    f32x4 acc[4][4], accd[4][4];
    {
        const u32 lane = lane_id(), r = lane & 15, g = lane >> 4;
        u32 pa0 = K2A[0] + wm * 8192u + r * 128u + ((g ^ (r >> 1)) << 4), pa1 = pa0 ^ 64u, pf = K2A[0] + wm * 8192u + lane * 16u, pf1 = pf + 1024u;
        u32 pb0 = wn * 8192u + lane * 16u, pb1 = pb0 + K1_Q;
#pragma unroll
        for (int n = 0; n < 4; ++n)
#pragma unroll
            for (int m = 0; m < 4; ++m) { acc[n][m] = (f32x4){0.f, 0.f, 0.f, 0.f}; accd[n][m] = acc[n][m]; asm volatile("" : "+v"(acc[n][m]), "+v"(accd[n][m])); }
        u32 fov = F_H - F_O;
        int jpub = wid * 16;
        const char* pfl = ws + F_O + (tm * 16 + tn) * 128;
        u32x2 tag2 = {(u32)tag, (u32)(tag >> 32)};
        asm volatile("" : "+v"(pa0), "+v"(pa1), "+v"(pf), "+v"(pf1), "+v"(pb0), "+v"(pb1), "+v"(fov), "+v"(tag2));
        const u32 q4 = r >> 2, p4 = lane & 3u, f = q4 | ((g & 1u) << 2);
        u32 tb0[4], tb1[4];
#pragma unroll
        for (int i = 0; i < 4; ++i) { tb0[i] = (8u * g + q4) * 256u + (((4u * wn + i) ^ f) << 5) + p4 * 8u; tb1[i] = tb0[i] + K1_Q; asm volatile("" : "+v"(tb0[i]), "+v"(tb1[i])); }
        bf16x8 A[2][4], B[2][4];
#define MM(n, m, h) do { mfma_acc(accd[n][m], B[h][n], A[h][m]); SCHED(); } while (0)
#define RDA(m, h, s, p0, p1) do { A[h][m] = lds128(((h) ? (p1) : (p0)) + (K2A[s] - K2A[0]) + (m) * 2048u); SCHED(); } while (0)
#define RDB(n, h, s) do { B[h][n] = lds128(((s) == 0 ? pb0 : pb1 + (K2B[s] - K1_Q)) + (n) * 2048u + (h) * 1024u); SCHED(); } while (0)
#define NEXTA(s, p0, p1) RDA(0, 0, s, p0, p1), RDA(0, 1, s, p0, p1), RDA(1, 1, s, p0, p1), RDA(1, 0, s, p0, p1), RDA(2, 0, s, p0, p1), RDA(2, 1, s, p0, p1), RDA(3, 1, s, p0, p1), RDA(3, 0, s, p0, p1)
#define STEP2(s, p0, p1, U1, SYNC)                                                                                          \
    GROUP(0, 0, 1, 2, 3, RDB(1, 0, s), RDB(1, 1, s), RDB(2, 0, s), RDB(2, 1, s), NIL, NIL, NIL, NIL)                        \
    GROUP(1, 3, 2, 1, 0, RDB(3, 0, s), RDB(3, 1, s), UPD(U1), NIL, NIL, NIL, NIL, NIL)                                      \
    wait_lgkm0(); SCHED(); SYNC; SCHED();                                                                                   \
    GROUP(2, 3, 2, 1, 0, RDB(0, 0, ((s) + 1) & 3), RDB(0, 1, ((s) + 1) & 3), NIL, NIL, NIL, NIL, NIL, NIL)                  \
    GROUPX(3, 0, 1, 2, 3, NEXTA(((s) + 1) & 3, p0, p1))
        wait_lgkm0();
        bar();
        RDB(0, 0, 0); RDB(0, 1, 0);
        RDA(0, 0, 0, pa0, pa1); RDA(0, 1, 0, pa0, pa1); RDA(1, 1, 0, pa0, pa1); RDA(1, 0, 0, pa0, pa1); RDA(2, 0, 0, pa0, pa1); RDA(2, 1, 0, pa0, pa1); RDA(3, 1, 0, pa0, pa1); RDA(3, 0, 0, pa0, pa1);
        int j = 0, jend = 4;
        asm volatile("" : "+s"(jend));
#pragma unroll 1
        for (;;) {
#pragma unroll 1
            do {
                u32 pn0, pn1;
                STEP2(0, pa0, pa1, NIL, bar())
                soft_idle<4, 2>(j);
                STEP2(1, pa0, pa1, NIL, { wait_vm<0>(); bar(); })
                STEP2(2, pa0, pa1, store_word_if(j, jpub, fov, tag2, pfl), bar())
                STEP2(3, pn0, pn1, { pn0 = j == 7 ? pf : pa0; pn1 = j == 7 ? pf1 : pa1; asm volatile("" : "+v"(pn0), "+v"(pn1)); }, bar())
            } while (++j != jend);
            if (j == 8) break;
            {
                const u32 ln = lane_id(), r1 = ln & 15, g1 = ln >> 4, hs = (mh ? HS0 : HS1) + hh * 2048u + ln * 16u;
                const bf16x8 Kp = lds128(hs);
                u32x4 vp = *(const LAS u32x4*)(size_t)(hs + 1024u);
                const bool first = (tm | mh) == 0;
                if (first) {
                    const u32 t0 = __builtin_amdgcn_ds_bpermute(r1 * 4u, V1[0][0]) & 0xffffu, t1 = __builtin_amdgcn_ds_bpermute(r1 * 4u, V1[1][0]) & 0xffffu;
                    vp = (u32x4){t0 * 0x10001u, t0 * 0x10001u, t1 * 0x10001u, t1 * 0x10001u};
                }
                const u32x4 o1 = window_attn(Q1, Kp, K1f, cat((u32x2){vp[0], vp[1]}, V1[0]), cat((u32x2){vp[2], vp[3]}, V1[1]), first, r1, g1);
                __builtin_amdgcn_raw_buffer_store_b128(o1, rws, ((((g1 & 1u) << 1) | (g1 >> 1)) * 16u + r1) * 16u, WS_O + (u32)(8 * tm + 4 * mh) * OTS + (u32)(2 * tn + hh) * 1024u, SC1);
                asm volatile("s_nop 7" ::: "memory");
                SCHED();
            }
            jend = 8; jpub += 4; fov = 0u;
        }
        {
            s16x4 T[2][4][2];
#undef MM
#define MM(n, m, h) do { mfma_acc(acc[n][m], tr_cat(T[h][n][0], T[h][n][1]), A[h][m]); SCHED(); } while (0)
#define RDT(n, h, t, s) do { T[h][n][t] = ldstr(((s) == 0 ? tb0[n] : tb1[n] + (K2B[s] - K1_Q)) + (h) * 8192u + (t) * 1024u); SCHED(); } while (0)
#define RDT4(n, s) RDT(n, 0, 0, s), RDT(n, 0, 1, s), RDT(n, 1, 0, s), RDT(n, 1, 1, s)
#define STEP3(s, SYNC)                                                                                                      \
    GROUPX(0, 0, 1, 2, 3, RDT4(1, s), RDT4(2, s))                                                                           \
    GROUPX(1, 3, 2, 1, 0, RDT4(3, s), NIL, NIL, NIL, NIL)                                                                   \
    wait_lgkm0(); SCHED(); SYNC; SCHED();                                                                                   \
    GROUPX(2, 3, 2, 1, 0, RDT4(0, ((s) + 1) & 3), NIL, NIL, NIL, NIL)                                                       \
    GROUPX(3, 0, 1, 2, 3, NEXTA(((s) + 1) & 3, pf, pf + 1024u))
            RDT(0, 0, 0, 0); RDT(0, 0, 1, 0); RDT(0, 1, 0, 0); RDT(0, 1, 1, 0);
#pragma unroll 1
            for (int j = 0; j < 2; ++j) { STEP3(0, bar()) STEP3(1, bar()) STEP3(2, bar()) STEP3(3, bar_unless(j, 1)) }
        }
        mfma_drain();
    }
    const u32 lane = lane_id(), r = lane & 15, g = lane >> 4;
    float gt[4], gd[4];
#pragma unroll
    for (int m = 0; m < 4; ++m) { gt[m] = *(const LAS float*)(size_t)(HS0 + (64 * wm + 16 * (m ^ (2 - 2 * hf)) + r) * 4u); gd[m] = rbf(hf ? gt[m] - 1.f : 1.f - gt[m]); }
#pragma unroll
    for (int n = 0; n < 4; ++n) {
        const u32x2 p0 = pk4(acc[n][0] * gt[0] + accd[n][0] * gd[0]), p1 = pk4(acc[n][1] * gt[1] + accd[n][1] * gd[1]);
        const u32x4 v = {p0[0], p0[1], p1[0], p1[1]};
        __builtin_amdgcn_raw_buffer_store_b128(v, rws, wid * 4096u + lane * 16u + n * 1024u, WS_X + (u32)b * 16384u, SC1);
    }
    SCHED();
    f32x4 keepv[4][2];
#pragma unroll
    for (int n = 0; n < 4; ++n) {
        keepv[n][0] = acc[n][2] * gt[2] + accd[n][2] * gd[2]; keepv[n][1] = acc[n][3] * gt[3] + accd[n][3] * gd[3];
        asm volatile("" : "+v"(keepv[n][0]), "+v"(keepv[n][1]));
    }
    wait_vm<0>();
    if (lane == 0) __hip_atomic_store((GAS u64*)(ws + F_X + (b * 4 + wid) * 128), tag, __ATOMIC_RELAXED, __HIP_MEMORY_SCOPE_AGENT);
    poll_ptr(ws + F_X + ((b ^ 4) * 4 + wid) * 128, tag);
#pragma unroll
    for (int n = 0; n < 4; ++n) {
        const u32x4 v = __builtin_amdgcn_raw_buffer_load_b128(rws, wid * 4096u + lane * 16u + n * 1024u, WS_X + (u32)(b ^ 4) * 16384u, SC1);
        keepv[n][0] += (f32x4){bf2f(v[0] & 0xffffu), __uint_as_float(v[0] & 0xffff0000u), bf2f(v[1] & 0xffffu), __uint_as_float(v[1] & 0xffff0000u)};
        keepv[n][1] += (f32x4){bf2f(v[2] & 0xffffu), __uint_as_float(v[2] & 0xffff0000u), bf2f(v[3] & 0xffffu), __uint_as_float(v[3] & 0xffff0000u)};
    }
    const u32 gp = ((g & 1u) << 1) | (g >> 1);
    const rsrc_t rout = mk_rsrc(out, 2048u * 2048u);
    const u32 ob = (u32)(128 * um + 64 * wm + 32 * hf) * 2048u + (u32)(128 * un + 64 * wn) * 2u, o2 = (r & 7u) * 2048u + (r >> 3) * 64u + gp * 16u;
#pragma unroll
    for (int m = 0; m < 2; ++m) {
        u32x4 c0 = widen(pk4(keepv[0][m]), pk4(keepv[1][m])), c1 = widen(pk4(keepv[2][m]), pk4(keepv[3][m]));
        const bool up = r & 8u;
#pragma unroll
        for (int j = 0; j < 4; ++j) {
            const u32 rv = __builtin_amdgcn_update_dpp(0u, up ? c0[j] : c1[j], 0x128, 0xf, 0xf, false);
            c0[j] = up ? rv : c0[j]; c1[j] = up ? c1[j] : rv;
        }
        __builtin_amdgcn_raw_buffer_store_b128(c0, rout, o2, ob + m * 32768u, SC1);
        __builtin_amdgcn_raw_buffer_store_b128(c1, rout, o2, ob + m * 32768u + 16384u, SC1);
    }
    asm volatile(".Lpc_end:");
}

extern "C" void kernel_launch(void* const* d_in, const int* in_sizes, int n_in, void* d_out, int out_size, void* d_ws, size_t ws_size, hipStream_t stream) {
    const u16* x = (const u16*)d_in[0]; const u16* wqkv = (const u16*)d_in[1]; const u16* wo = (const u16*)d_in[2];
    const u16* cr = (const u16*)d_in[4]; const u16* ci = (const u16*)d_in[5]; const u16* wre = (const u16*)d_in[6];
    const u16* wim = (const u16*)d_in[7]; const u16* gw = (const u16*)d_in[8];
    (void)hipFuncSetAttribute((const void*)fused, hipFuncAttributeMaxDynamicSharedMemorySize, (int)LDS_BYTES);
    fused<<<256, 512, LDS_BYTES, stream>>>(x, wqkv, wo, cr, ci, wre, wim, gw, (char*)d_ws, (u16*)d_out);
}
```
